```python
import torch

def _softmax(x, axis=-1):
    return torch.softmax(x, dim=axis)
def _gelu(x):
    return torch.nn.functional.gelu(x, approximate="tanh")
def _silu(x):
    return torch.nn.functional.silu(x)
def _sigmoid(x):
    return torch.sigmoid(x)
def _layernorm(x, axis=-1, eps=1e-5):
    m = x.mean(dim=axis, keepdim=True)
    v = x.var(dim=axis, keepdim=True, unbiased=False)
    return (x - m) / torch.sqrt(v + eps)
def _scatter_add(tgt, idx, src, axis=0):
    return tgt.clone().index_add_(axis, idx, src)
def _segment_sum(src, seg, n):
    out = torch.zeros((n,) + src.shape[1:], dtype=src.dtype)
    return out.index_add_(0, seg, src)
def _pad(x, pw):
    flat = tuple(v for pair in reversed(pw) for v in pair)
    return torch.nn.functional.pad(x, flat)

kernel_name = 'standalone__gqa_softmax__near_ridge'

def setup_inputs():

    _w = (lambda a: (lambda t: t.to(torch.bfloat16) if t.is_floating_point() else t)(torch.as_tensor(a)))
    B, Hq, Hkv, Sq, Sk, D = 8, 32, 32, 256, 1024, 128
    return {
        "q": _w((lambda *s: torch.randn(*s, dtype=torch.bfloat16) * 0.8)(B, Hq, Sq, D)),
        "k": _w((lambda *s: torch.randn(*s, dtype=torch.bfloat16) * 0.8)(B, Hkv, Sk, D)),
        "v": _w((lambda *s: torch.randn(*s, dtype=torch.bfloat16) * 0.05)(B, Hkv, Sk, D)),
    }

def reference(q, k, v):
    B, Hq, Sq, D = q.shape
    Hkv = k.shape[1]
    rep = Hq // Hkv
    k = (lambda x, n, axis: torch.repeat_interleave(x, n, dim=axis))(k, rep, axis=1)
    v = (lambda x, n, axis: torch.repeat_interleave(x, n, dim=axis))(v, rep, axis=1)
    scale = D ** -0.5
    scores = torch.einsum("bhqd,bhkd->bhqk", q, k) * scale
    p = _softmax(scores, axis=-1)
    return torch.einsum("bhqk,bhkd->bhqd", p, v)
```

```cpp
#include <hip/hip_runtime.h>
#include <hip/hip_bf16.h>
#include <cstdio>

using bf16 = __hip_bfloat16;
constexpr int   D = 128, NW = 8, QBLK = 32, KVBLK = 64;
constexpr float SCALE = 0.088388347648318440f;
#define ATTN_THR 8.f
constexpr float THR = ATTN_THR;
#define ATTN_LAYOUT 0
constexpr int LAYOUT = ATTN_LAYOUT;
#define ATTN_SDEPTH 2
constexpr int SDEPTH = ATTN_SDEPTH;
static_assert(SDEPTH == 1 || SDEPTH == 2, "SDEPTH is 1 or 2");
#define ATTN_B 8
#define ATTN_HQ 32
#define ATTN_HKV 32
#define ATTN_S 256
#define ATTN_SKV 1024
constexpr int B = ATTN_B, HQ = ATTN_HQ, HKV = ATTN_HKV, S = ATTN_S, SKV = ATTN_SKV;
constexpr int LDQ = LAYOUT ? HQ * D : D, LDK = LAYOUT ? HKV * D : D, LDO = LDQ;
static_assert(S % (QBLK * NW) == 0 && SKV % (2 * KVBLK) == 0 && HQ % HKV == 0, "S % 256 == 0, SKV % 128 == 0 (even tile count), HQ % HKV == 0");
constexpr size_t SHM_V = KVBLK * D * 2, SHM_K = KVBLK * D * 2, SHM_ATTN = 2 * SHM_V + 2 * SHM_K + NW * 64 * 4;

using bf16x8 = __attribute__((ext_vector_type(8))) short;
using s16x4  = __attribute__((ext_vector_type(4))) short;
using f32x16 = __attribute__((ext_vector_type(16))) float;
using f32x8  = __attribute__((ext_vector_type(8))) float;
using u32x4  = __attribute__((ext_vector_type(4))) unsigned;
#define KSWZ(row, colB) ((row) * 256 + ((colB) ^ (((row) & 7) << 4)))
#define SBAR() __builtin_amdgcn_sched_barrier(0)
__device__ __forceinline__ int crow(int r, int hi) { return (r & 3) + 8 * (r >> 2) + 4 * hi; }
__device__ __forceinline__ unsigned cvtpk(float lo, float hi) {
  unsigned r; asm volatile("v_cvt_pk_bf16_f32 %0, %1, %2" : "=v"(r) : "v"(lo), "v"(hi)); return r;
}
template <typename TIn> struct Stage;
template <> struct Stage<bf16>  { using T = bf16x8;
  __device__ static __forceinline__ T ld8(const bf16* p) { return *reinterpret_cast<const bf16x8*>(p); }
  __device__ static __forceinline__ bf16x8 tobf(T x) { return x; } };
template <> struct Stage<float> { using T = f32x8;
  __device__ static __forceinline__ T ld8(const float* p) { return *reinterpret_cast<const f32x8*>(p); }
  __device__ static __forceinline__ bf16x8 tobf(T x) {
    u32x4 w = {cvtpk(x[0], x[1]), cvtpk(x[2], x[3]), cvtpk(x[4], x[5]), cvtpk(x[6], x[7])}; return *reinterpret_cast<bf16x8*>(&w); } };

__device__ __forceinline__ void partialSM(f32x16& p0, f32x16& p1, float& m_reg, float& mn, float& alpha) {
  constexpr float C = SCALE * 1.4426950408889634f;
  float pmax = p0[0]; for (int r = 1; r < 16; ++r) pmax = fmaxf(pmax, p0[r]); for (int r = 0; r < 16; ++r) pmax = fmaxf(pmax, p1[r]);
  { auto rr = __builtin_amdgcn_permlane32_swap(__float_as_uint(pmax), __float_as_uint(pmax), false, false);
    pmax = fmaxf(__uint_as_float(rr[0]), __uint_as_float(rr[1])); }
  if (__builtin_expect(__all(pmax - m_reg <= THR / SCALE), 1)) { mn = m_reg; alpha = 1.f; }
  else { mn = fmaxf(m_reg, pmax); alpha = __builtin_amdgcn_exp2f((m_reg - mn) * C); m_reg = mn; }
  float mnC = -mn * C;
  for (int r = 0; r < 16; ++r) p0[r] = fmaf(p0[r], C, mnC); for (int r = 0; r < 16; ++r) p1[r] = fmaf(p1[r], C, mnC);
  for (int r = 0; r < 16; ++r) p0[r] = __builtin_amdgcn_exp2f(p0[r]);
}
__device__ __forceinline__ void finishSM(f32x16& p0, f32x16& p1, float alpha, float& l_reg, bf16x8& pa0, bf16x8& pa1, bf16x8& pa2, bf16x8& pa3) {
  for (int r = 0; r < 16; ++r) p1[r] = __builtin_amdgcn_exp2f(p1[r]);
  float ps = 0; for (int r = 0; r < 16; ++r) ps += p0[r]; for (int r = 0; r < 16; ++r) ps += p1[r];
  { auto rr = __builtin_amdgcn_permlane32_swap(__float_as_uint(ps), __float_as_uint(ps), false, false);
    ps = __uint_as_float(rr[0]) + __uint_as_float(rr[1]); }
  l_reg = l_reg * alpha + ps;
#define PK4(P, BASE, OUT) do { unsigned a0 = cvtpk(P[BASE + 0], P[BASE + 1]), a1 = cvtpk(P[BASE + 2], P[BASE + 3]);   \
    unsigned b0 = cvtpk(P[BASE + 4], P[BASE + 5]), b1 = cvtpk(P[BASE + 6], P[BASE + 7]);                              \
    auto r0 = __builtin_amdgcn_permlane32_swap(a0, b0, false, false); auto r1 = __builtin_amdgcn_permlane32_swap(a1, b1, false, false); \
    u32x4 w = {r0[0], r1[0], r0[1], r1[1]}; OUT = *reinterpret_cast<bf16x8*>(&w); } while (0)
  PK4(p0, 0, pa0); PK4(p0, 8, pa1); PK4(p1, 0, pa2); PK4(p1, 8, pa3);
#undef PK4
}
__device__ __forceinline__ void qkt(f32x16& p0, f32x16& p1, const bf16* Ks, const bf16x8* qr, int r32, int hi) {
  p0 = f32x16{}; p1 = f32x16{};
  for (int d0 = 0; d0 < 8; ++d0) { int cb = (d0 * 16 + hi * 8) * 2;
    bf16x8 b0 = *reinterpret_cast<const bf16x8*>((const char*)Ks + KSWZ(r32, cb));
    bf16x8 b1 = *reinterpret_cast<const bf16x8*>((const char*)Ks + KSWZ(32 + r32, cb));
    p0 = __builtin_amdgcn_mfma_f32_32x32x16_bf16(b0, qr[d0], p0, 0, 0, 0);
    p1 = __builtin_amdgcn_mfma_f32_32x32x16_bf16(b1, qr[d0], p1, 0, 0, 0); }
}
__device__ __forceinline__ int v_st(int k, int c) { const int kk = (k & ~0xC) | ((k & 4) << 1) | ((k & 8) >> 1); return ((kk >> 3) * 4 + (c >> 5)) * 512 + ((kk & 7) * 32 + (c & 31)) * 2; }
__device__ __forceinline__ int v_rd_base(int lane) { return ((lane & 3) << 3) | (((lane >> 2) & 3) << 6) | (((lane >> 4) & 1) << 5) | (((lane >> 5) & 1) << 8); }
constexpr int v_rd_off(int d0, int ks, int half) { return d0 * 512 + ks * 4096 + half * 2048; }
template <int OFF> __device__ __forceinline__ s16x4 tr_read(int vb) {
  s16x4 r; asm volatile("ds_read_b64_tr_b16 %0, %1 offset:%2" : "=&v"(r) : "v"(vb), "i"(OFF) : "memory"); return r;
}
template <int D0> __device__ __forceinline__ void pv_one(f32x16& od, int vb, bf16x8 pa0, bf16x8 pa1, bf16x8 pa2, bf16x8 pa3) {
  const s16x4 l0 = tr_read<v_rd_off(D0, 0, 0)>(vb), h0 = tr_read<v_rd_off(D0, 0, 1)>(vb), l1 = tr_read<v_rd_off(D0, 1, 0)>(vb), h1 = tr_read<v_rd_off(D0, 1, 1)>(vb);
  const s16x4 l2 = tr_read<v_rd_off(D0, 2, 0)>(vb), h2 = tr_read<v_rd_off(D0, 2, 1)>(vb), l3 = tr_read<v_rd_off(D0, 3, 0)>(vb), h3 = tr_read<v_rd_off(D0, 3, 1)>(vb);
  asm volatile("s_waitcnt lgkmcnt(0)" ::: "memory"); SBAR();
#define PK(L, H) (bf16x8){L[0], L[1], L[2], L[3], H[0], H[1], H[2], H[3]}
  od = __builtin_amdgcn_mfma_f32_32x32x16_bf16(pa0, PK(l0, h0), od, 0, 0, 0);
  od = __builtin_amdgcn_mfma_f32_32x32x16_bf16(pa1, PK(l1, h1), od, 0, 0, 0);
  od = __builtin_amdgcn_mfma_f32_32x32x16_bf16(pa2, PK(l2, h2), od, 0, 0, 0);
  od = __builtin_amdgcn_mfma_f32_32x32x16_bf16(pa3, PK(l3, h3), od, 0, 0, 0);
#undef PK
}
__device__ __forceinline__ void pv_d0(f32x16* o, int vb, bf16x8 pa0, bf16x8 pa1, bf16x8 pa2, bf16x8 pa3) {
  pv_one<0>(o[0], vb, pa0, pa1, pa2, pa3); pv_one<1>(o[1], vb, pa0, pa1, pa2, pa3); pv_one<2>(o[2], vb, pa0, pa1, pa2, pa3); pv_one<3>(o[3], vb, pa0, pa1, pa2, pa3);
}

template <typename TQ>
__device__ __forceinline__ void attn_dense_body(const TQ* __restrict__ Qb, const bf16* __restrict__ Kh, const bf16* __restrict__ Vh,
                                                bf16* __restrict__ Ob, int seq, char* lds) {
  using St = Stage<bf16>; using SQ = Stage<TQ>;
  const int tid = threadIdx.x, wid = tid >> 6, lane = tid & 63, r32 = lane & 31, hi = lane >> 5;
  bf16* V_lds = (bf16*)lds; bf16* K_lds = (bf16*)(lds + 2 * SHM_V);
  float* ws = (float*)(lds + 2 * SHM_V + 2 * SHM_K) + wid * 64; float* li_l = ws; float* al_l = ws + 32;
  float m_reg = -1e30f, l_reg = 0; f32x16 o[4] = {}; bf16x8 qr[8];
  const TQ* Qw = Qb + (long)(wid * QBLK + r32) * LDQ + hi * 8;
#pragma unroll
  for (int d0 = 0; d0 < 8; ++d0) qr[d0] = SQ::tobf(SQ::ld8(Qw + d0 * 16));
  const int sr = tid >> 4, sc = (tid & 15) * 8, vst0 = v_st(sr, sc), vst1 = v_st(32 + sr, sc);
  const int vb0 = (int)(uintptr_t)V_lds + v_rd_base(lane);
  struct { typename St::T vs0, vs1, ks0, ks1; } sr_[SDEPTH];
#define SLOAD(i, k0) do { sr_[i].vs0 = St::ld8(&Vh[(long)((k0) + sr) * LDK + sc]); sr_[i].vs1 = St::ld8(&Vh[(long)((k0) + 32 + sr) * LDK + sc]); \
    sr_[i].ks0 = St::ld8(&Kh[(long)((k0) + sr) * LDK + sc]); sr_[i].ks1 = St::ld8(&Kh[(long)((k0) + 32 + sr) * LDK + sc]); } while (0)
#define SWRITE(b, i) do { *(bf16x8*)((char*)V_lds + (b) * SHM_V + vst0) = St::tobf(sr_[i].vs0);          \
    *(bf16x8*)((char*)V_lds + (b) * SHM_V + vst1) = St::tobf(sr_[i].vs1); int kc = sc * 2;               \
    *(bf16x8*)((char*)K_lds + (b) * SHM_K + KSWZ(sr, kc)) = St::tobf(sr_[i].ks0);                       \
    *(bf16x8*)((char*)K_lds + (b) * SHM_K + KSWZ(32 + sr, kc)) = St::tobf(sr_[i].ks1); } while (0)
#define SWAIT() do { if constexpr (SDEPTH == 2) asm volatile("s_waitcnt vmcnt(4)" ::: "memory"); else asm volatile("s_waitcnt vmcnt(0)" ::: "memory"); } while (0)
#define RESC(a) do { if (__any((a) < 1.f)) { if (hi == 0) al_l[r32] = (a); asm volatile("s_waitcnt lgkmcnt(0)" ::: "memory"); \
    for (int d = 0; d < 4; ++d) for (int r = 0; r < 16; ++r) o[d][r] *= al_l[crow(r, hi)]; } } while (0)
  f32x16 pA0, pA1, pB0, pB1; float mnA, mnB, alA, alB; bf16x8 pa0, pa1, pa2, pa3; const int NT = seq / KVBLK;
  constexpr int SE = 0, SO = SDEPTH - 1;
  SLOAD(SE, 0); asm volatile("s_waitcnt vmcnt(0)" ::: "memory"); SWRITE(0, SE); __syncthreads();
  qkt(pA0, pA1, K_lds, qr, r32, hi); partialSM(pA0, pA1, m_reg, mnA, alA);
  SLOAD(SO, KVBLK); if constexpr (SDEPTH == 2) { if (2 < NT) SLOAD(SE, 2 * KVBLK); }
  SWAIT(); SWRITE(1, SO); __syncthreads();
  for (int j = 1; j + 1 < NT; j += 2) {
    SBAR(); qkt(pB0, pB1, (bf16*)((char*)K_lds + SHM_K), qr, r32, hi);
    finishSM(pA0, pA1, alA, l_reg, pa0, pa1, pa2, pa3); SBAR();
    SLOAD(SO, (j + SDEPTH) * KVBLK); SBAR();
    pv_d0(o, vb0, pa0, pa1, pa2, pa3); partialSM(pB0, pB1, m_reg, mnB, alB);
    __syncthreads(); SWAIT(); SWRITE(0, SE);
    RESC(alB); __syncthreads();
    SBAR(); qkt(pA0, pA1, K_lds, qr, r32, hi);
    finishSM(pB0, pB1, alB, l_reg, pa0, pa1, pa2, pa3); SBAR();
    if (SDEPTH == 1 || j + 3 < NT) SLOAD(SE, (j + 1 + SDEPTH) * KVBLK); SBAR();
    pv_d0(o, vb0 + (int)SHM_V, pa0, pa1, pa2, pa3); partialSM(pA0, pA1, m_reg, mnA, alA);
    __syncthreads(); SWAIT(); SWRITE(1, SO);
    RESC(alA); __syncthreads();
  }
  SBAR(); qkt(pB0, pB1, (bf16*)((char*)K_lds + SHM_K), qr, r32, hi);
  finishSM(pA0, pA1, alA, l_reg, pa0, pa1, pa2, pa3); SBAR();
  pv_d0(o, vb0, pa0, pa1, pa2, pa3); partialSM(pB0, pB1, m_reg, mnB, alB);
  __syncthreads(); RESC(alB);
  finishSM(pB0, pB1, alB, l_reg, pa0, pa1, pa2, pa3); SBAR();
  pv_d0(o, vb0 + (int)SHM_V, pa0, pa1, pa2, pa3);
  if (hi == 0) li_l[r32] = l_reg; asm volatile("s_waitcnt lgkmcnt(0)" ::: "memory");
  float rli[16];
#pragma unroll
  for (int r = 0; r < 16; ++r) rli[r] = __builtin_amdgcn_rcpf(li_l[crow(r, hi)]);
  bf16* Ow = Ob + (long)(wid * QBLK) * LDO;
#pragma unroll
  for (int r = 0; r < 16; ++r) { int orow = crow(r, hi);
    for (int d0 = 0; d0 < 4; ++d0) Ow[(long)orow * LDO + d0 * 32 + r32] = __float2bfloat16(o[d0][r] * rli[r]); }
#undef SLOAD
#undef SWRITE
#undef SWAIT
#undef RESC
}

template <typename TQ>
__global__ __launch_bounds__(64 * NW, 1)
void attn_fwd_dense(const TQ* __restrict__ Q, const bf16* __restrict__ K, const bf16* __restrict__ V, bf16* __restrict__ O, int sq, int skv, int GQA) {
  extern __shared__ __attribute__((aligned(16))) char shm[];
  const long b = blockIdx.z, h = blockIdx.y, Hq = gridDim.y, Hkv = Hq / GQA, kvh = h / GQA, i0 = (long)blockIdx.x * (QBLK * NW);
  if constexpr (LAYOUT == 0) {
    const long q0 = ((b * Hq + h) * sq + i0) * D, k0 = (b * Hkv + kvh) * skv * (long)D;
    attn_dense_body<TQ>(Q + q0, K + k0, V + k0, O + q0, skv, shm);
  } else {
    const long q0 = ((b * sq + i0) * HQ + h) * (long)D, k0 = (b * skv * HKV + kvh) * (long)D;
    attn_dense_body<TQ>(Q + q0, K + k0, V + k0, O + q0, skv, shm);
  }
}

extern "C" void kernel_launch(void* const* d_in, const int* in_sizes, int n_in, void* d_out, int out_size, void* d_ws, size_t ws_size, hipStream_t stream) {
  (void)hipFuncSetAttribute((const void*)attn_fwd_dense<bf16>, hipFuncAttributeMaxDynamicSharedMemorySize, (int)SHM_ATTN);
  hipLaunchKernelGGL(attn_fwd_dense<bf16>, dim3(S / (QBLK * NW), HQ, B), dim3(64 * NW), SHM_ATTN, stream,
                     (const bf16*)d_in[0], (const bf16*)d_in[1], (const bf16*)d_in[2], (bf16*)d_out, S, SKV, HQ / HKV);
}
```

```cpp
#include <hip/hip_runtime.h>
#include <hip/hip_bf16.h>


typedef float f32x4_t __attribute__((ext_vector_type(4)));
typedef __bf16 bf16x8_t __attribute__((ext_vector_type(8)));
typedef unsigned u32x4_t __attribute__((ext_vector_type(4)));
typedef unsigned u32x2_t __attribute__((ext_vector_type(2)));
typedef __bf16 bf16x2_t __attribute__((ext_vector_type(2)));
typedef float f32x2_t __attribute__((ext_vector_type(2)));

__device__ __forceinline__ unsigned pack_bf16(float lo, float hi) {
  f32x2_t v = {lo, hi};
  return __builtin_bit_cast(unsigned, __builtin_convertvector(v, bf16x2_t));
}

__device__ __forceinline__ void redo_rows(const unsigned short* __restrict__ q, const unsigned short* __restrict__ k,
                                               const unsigned short* __restrict__ v, unsigned short* __restrict__ o, int lane) {
  const int r = lane & 15, g = lane >> 4;
  const float c = 0.12751743082459868f;
  bf16x8_t qf[4];
#pragma unroll
  for (int ds = 0; ds < 4; ++ds) qf[ds] = *reinterpret_cast<const bf16x8_t*>(q + r * 128 + 32 * ds + 8 * g);
  f32x4_t acc[8];
#pragma unroll
  for (int db = 0; db < 8; ++db) acc[db] = f32x4_t{0.f, 0.f, 0.f, 0.f};
  float m = -__builtin_inff(), l = 0.f;
#pragma unroll 1
  for (int k0 = 0; k0 < 1024; k0 += 32) {
    f32x4_t s0 = {0.f, 0.f, 0.f, 0.f}, s1 = {0.f, 0.f, 0.f, 0.f};
#pragma unroll
    for (int ds = 0; ds < 4; ++ds) {
      const bf16x8_t a0 = *reinterpret_cast<const bf16x8_t*>(k + (k0 + r) * 128 + 32 * ds + 8 * g);
      const bf16x8_t a1 = *reinterpret_cast<const bf16x8_t*>(k + (k0 + 16 + r) * 128 + 32 * ds + 8 * g);
      s0 = __builtin_amdgcn_mfma_f32_16x16x32_bf16(a0, qf[ds], s0, 0, 0, 0);
      s1 = __builtin_amdgcn_mfma_f32_16x16x32_bf16(a1, qf[ds], s1, 0, 0, 0);
    }
    float x[8];
#pragma unroll
    for (int j = 0; j < 4; ++j) { x[j] = s0[j] * c; x[4 + j] = s1[j] * c; }
    float mx = x[0];
#pragma unroll
    for (int j = 1; j < 8; ++j) mx = fmaxf(mx, x[j]);
    mx = fmaxf(mx, __shfl_xor(mx, 16, 64));
    mx = fmaxf(mx, __shfl_xor(mx, 32, 64));
    const float mn = fmaxf(m, mx);
    const float ms = (mn == -__builtin_inff()) ? 0.f : mn;
    const float alpha = __builtin_amdgcn_exp2f(m - ms);
    float ps = 0.f;
#pragma unroll
    for (int j = 0; j < 8; ++j) { x[j] = __builtin_amdgcn_exp2f(x[j] - ms); ps += x[j]; }
    l = l * alpha + ps;
    m = mn;
    const u32x4_t pw = {pack_bf16(x[0], x[1]), pack_bf16(x[2], x[3]), pack_bf16(x[4], x[5]), pack_bf16(x[6], x[7])};
    const bf16x8_t pf = __builtin_bit_cast(bf16x8_t, pw);
#pragma unroll
    for (int db = 0; db < 8; ++db) {
      const unsigned short* vp = v + (k0 + 4 * g) * 128 + 16 * db + r;
      u32x4_t w;
      w[0] = vp[0] | ((unsigned)vp[128] << 16);
      w[1] = vp[256] | ((unsigned)vp[384] << 16);
      w[2] = vp[2048] | ((unsigned)vp[2176] << 16);
      w[3] = vp[2304] | ((unsigned)vp[2432] << 16);
      acc[db] *= alpha;
      acc[db] = __builtin_amdgcn_mfma_f32_16x16x32_bf16(__builtin_bit_cast(bf16x8_t, w), pf, acc[db], 0, 0, 0);
    }
  }
  l += __shfl_xor(l, 16, 64);
  l += __shfl_xor(l, 32, 64);
  const float inv = 1.0f / l;
#pragma unroll
  for (int db = 0; db < 8; ++db) {
    const u32x2_t w = {pack_bf16(acc[db][0] * inv, acc[db][1] * inv), pack_bf16(acc[db][2] * inv, acc[db][3] * inv)};
    *reinterpret_cast<u32x2_t*>(o + r * 128 + 16 * db + 4 * g) = w;
  }
}

__global__ __launch_bounds__(256, 1) void attn_fwd_kernel(const void* __restrict__ Q, const void* __restrict__ K, const void* __restrict__ V, void* __restrict__ O
) {
  __shared__ __attribute__((aligned(1024))) char lds[163840];
  unsigned flag;
  const unsigned ldsb = (unsigned)(size_t)lds;
  asm volatile(
    "s_memrealtime s[18:19]\n"
    "s_load_dwordx8 s[8:15], %[ka], 0x0\n"
    "v_readfirstlane_b32 s40, %[tid]\n"
    "v_and_b32 v9, 63, %[tid]\n"
    "s_lshl_b32 s41, %[bid], 16\n"
    "v_bfe_u32 v10, v9, 2, 2\n"
    "s_lshr_b32 s40, s40, 6\n"
    "v_lshrrev_b32 v11, 4, v9\n"
    "s_lshl_b32 s42, s40, 14\n"
    "v_and_b32 v12, 3, v9\n"
    "s_add_u32 s41, s41, s42\n"
    "v_lshlrev_b32 v5, 8, v10\n"
    "s_mov_b32 s30, 16384\n"
    "v_lshl_add_u32 v5, v11, 6, v5\n"
    "s_mov_b32 s31, 0x20000\n"
    "v_lshl_add_u32 v5, v12, 4, v5\n"
    "s_lshl_b32 s43, s40, 13\n"
    "v_xor_b32 v6, 16, v5\n"
    "v_xor_b32 v7, 32, v5\n"
    "v_xor_b32 v8, 48, v5\n"
    "s_movk_i32 s48, 0x1000\n"
    "s_add_u32 m0, s43, 0x8000\n"
    "s_movk_i32 s49, 0x2000\n"
    "s_lshl_b32 s37, s40, 12\n"
    "s_waitcnt lgkmcnt(0)\n"
    "s_add_u32 s28, s8, s41\n"
    "s_addc_u32 s29, s9, 0\n"
    "s_and_b32 s29, s29, 0xffff\n"
    "buffer_load_dwordx4 v5, s[28:31], 0 offen nt lds\n"
    "buffer_load_dwordx4 v7, s[28:31], 0 offen offset:1024 nt lds\n"
    "buffer_load_dwordx4 v8, s[28:31], 0 offen offset:2048 nt lds\n"
    "buffer_load_dwordx4 v6, s[28:31], 0 offen offset:3072 nt lds\n"
    "s_add_u32 s44, s14, s41\n"
    "s_addc_u32 s45, s15, 0\n"
    "s_lshl_b32 s41, %[bid], 18\n"
    "s_add_u32 s41, s41, s37\n"
    "s_add_u32 s20, s10, s41\n"
    "s_addc_u32 s21, s11, 0\n"
    "s_and_b32 s21, s21, 0xffff\n"
    "s_sub_u32 s22, 0x40000, s37\n"
    "s_mov_b32 s23, 0x20000\n"
    "s_mov_b32 m0, s37\n"
    "s_mov_b32 s36, 0\n"
    "buffer_load_dwordx4 v5, s[20:23], s36 offen nt lds\n"
    "buffer_load_dwordx4 v7, s[20:23], s36 offen offset:1024 nt lds\n"
    "buffer_load_dwordx4 v8, s[20:23], s36 offen offset:2048 nt lds\n"
    "buffer_load_dwordx4 v6, s[20:23], s36 offen offset:3072 nt lds\n"
    "s_add_u32 m0, s43, 0x9000\n"
    "s_movk_i32 s50, 0x3000\n"
    "buffer_load_dwordx4 v5, s[28:31], s48 offen nt lds\n"
    "buffer_load_dwordx4 v7, s[28:31], s48 offen offset:1024 nt lds\n"
    "buffer_load_dwordx4 v8, s[28:31], s48 offen offset:2048 nt lds\n"
    "buffer_load_dwordx4 v6, s[28:31], s48 offen offset:3072 nt lds\n"
    "s_add_u32 s24, s12, s41\n"
    "s_addc_u32 s25, s13, 0\n"
    "s_and_b32 s25, s25, 0xffff\n"
    "s_mov_b32 s26, s22\n"
    "s_mov_b32 s27, 0x20000\n"
    "s_and_b32 s45, s45, 0xffff\n"
    "s_mov_b32 s46, 16384\n"
    "s_mov_b32 s47, 0x20000\n"
    "v_lshrrev_b32 v13, 1, v10\n"
    "v_xor_b32 v14, v10, v13\n"
    "v_and_b32 v14, 1, v14\n"
    "v_lshl_or_b32 v13, v14, 1, v13\n"
    "v_xor_b32 v13, v13, v11\n"
    "v_lshlrev_b32 v2, 10, v10\n"
    "v_lshl_add_u32 v2, v12, 6, v2\n"
    "v_lshl_add_u32 v2, v13, 4, v2\n"
    "v_lshlrev_b32 v3, 10, v11\n"
    "v_lshl_add_u32 v3, v10, 6, v3\n"
    "v_and_b32 v13, 1, v11\n"
    "v_lshl_add_u32 v3, v13, 5, v3\n"
    "v_lshl_add_u32 v3, v12, 3, v3\n"
    "v_or_b32 v3, 0x10000, v3\n"
    "v_xor_b32 v4, 32, v3\n"
    "v_cmp_eq_u32 vcc, 0, v12\n"
    "v_mov_b32 v13, 0x3f803f80\n"
    "s_add_u32 s42, s43, 0x8000\n"
    "v_cndmask_b32 v13, 0, v13, vcc\n"
    "v_add_u32 v56, s42, v2\n"
    "v_add_u32 v57, 0x10000, v56\n"
    "v_accvgpr_write_b32 a128, v13\n"
    "v_accvgpr_write_b32 a129, v13\n"
    "v_accvgpr_write_b32 a130, v13\n"
    "v_accvgpr_write_b32 a131, v13\n"
    "s_mov_b32 s38, 0x3e0293ee\n"
    "s_mov_b32 s39, 0x3e0293ee\n"
    "v_mov_b64 v[128:129], 0\n"
    "v_mov_b64 v[130:131], 0\n"
    "v_mov_b64 v[132:133], 0\n"
    "v_mov_b64 v[134:135], 0\n"
    "v_mov_b64 v[136:137], 0\n"
    "v_mov_b64 v[138:139], 0\n"
    "v_mov_b64 v[140:141], 0\n"
    "v_mov_b64 v[142:143], 0\n"
    "v_mov_b64 v[144:145], 0\n"
    "v_mov_b64 v[146:147], 0\n"
    "v_mov_b64 v[148:149], 0\n"
    "v_mov_b64 v[150:151], 0\n"
    "v_mov_b64 v[152:153], 0\n"
    "v_mov_b64 v[154:155], 0\n"
    "v_mov_b64 v[156:157], 0\n"
    "v_mov_b64 v[158:159], 0\n"
    "v_mov_b64 v[160:161], 0\n"
    "v_mov_b64 v[162:163], 0\n"
    "v_mov_b64 v[164:165], 0\n"
    "v_mov_b64 v[166:167], 0\n"
    "v_mov_b64 v[168:169], 0\n"
    "v_mov_b64 v[170:171], 0\n"
    "v_mov_b64 v[172:173], 0\n"
    "v_mov_b64 v[174:175], 0\n"
    "v_mov_b64 v[176:177], 0\n"
    "v_mov_b64 v[178:179], 0\n"
    "v_mov_b64 v[180:181], 0\n"
    "v_mov_b64 v[182:183], 0\n"
    "v_mov_b64 v[184:185], 0\n"
    "v_mov_b64 v[186:187], 0\n"
    "v_mov_b64 v[188:189], 0\n"
    "v_mov_b64 v[190:191], 0\n"
    "v_mov_b64 v[192:193], 0\n"
    "v_mov_b64 v[194:195], 0\n"
    "v_mov_b64 v[196:197], 0\n"
    "v_mov_b64 v[198:199], 0\n"
    "v_mov_b64 v[200:201], 0\n"
    "v_mov_b64 v[202:203], 0\n"
    "v_mov_b64 v[204:205], 0\n"
    "v_mov_b64 v[206:207], 0\n"
    "v_mov_b64 v[208:209], 0\n"
    "v_mov_b64 v[210:211], 0\n"
    "v_mov_b64 v[212:213], 0\n"
    "v_mov_b64 v[214:215], 0\n"
    "v_mov_b64 v[216:217], 0\n"
    "v_mov_b64 v[218:219], 0\n"
    "v_mov_b64 v[220:221], 0\n"
    "v_mov_b64 v[222:223], 0\n"
    "v_mov_b64 v[224:225], 0\n"
    "v_mov_b64 v[226:227], 0\n"
    "v_mov_b64 v[228:229], 0\n"
    "v_mov_b64 v[230:231], 0\n"
    "v_mov_b64 v[232:233], 0\n"
    "v_mov_b64 v[234:235], 0\n"
    "v_mov_b64 v[236:237], 0\n"
    "v_mov_b64 v[238:239], 0\n"
    "v_mov_b64 v[240:241], 0\n"
    "v_mov_b64 v[242:243], 0\n"
    "v_mov_b64 v[244:245], 0\n"
    "v_mov_b64 v[246:247], 0\n"
    "v_mov_b64 v[248:249], 0\n"
    "v_mov_b64 v[250:251], 0\n"
    "v_mov_b64 v[252:253], 0\n"
    "v_mov_b64 v[254:255], 0\n"
    "v_accvgpr_write_b32 a132, 0\n"
    "v_accvgpr_write_b32 a133, 0\n"
    "v_accvgpr_write_b32 a134, 0\n"
    "v_accvgpr_write_b32 a135, 0\n"
    "v_accvgpr_write_b32 a136, 0\n"
    "v_accvgpr_write_b32 a137, 0\n"
    "v_accvgpr_write_b32 a138, 0\n"
    "v_accvgpr_write_b32 a139, 0\n"
    "v_accvgpr_write_b32 a140, 0\n"
    "v_accvgpr_write_b32 a141, 0\n"
    "v_accvgpr_write_b32 a142, 0\n"
    "v_accvgpr_write_b32 a143, 0\n"
    "v_accvgpr_write_b32 a144, 0\n"
    "v_accvgpr_write_b32 a145, 0\n"
    "v_accvgpr_write_b32 a146, 0\n"
    "v_accvgpr_write_b32 a147, 0\n"
    "s_mov_b32 s51, 0xffff0000\n"
    "s_mov_b32 s35, 0x4000\n"
    "s_waitcnt vmcnt(8)\n"
    "ds_read_b128 v[64:67], v56\n"
    "ds_read_b128 v[68:71], v56 offset:256\n"
    "ds_read_b128 v[72:75], v56 offset:512\n"
    "ds_read_b128 v[76:79], v56 offset:768\n"
    "s_add_u32 m0, s43, 0x18000\n"
    "s_nop 0\n"
    "buffer_load_dwordx4 v5, s[28:31], s49 offen nt lds\n"
    "buffer_load_dwordx4 v7, s[28:31], s49 offen offset:1024 nt lds\n"
    "buffer_load_dwordx4 v8, s[28:31], s49 offen offset:2048 nt lds\n"
    "buffer_load_dwordx4 v6, s[28:31], s49 offen offset:3072 nt lds\n"
    "s_waitcnt lgkmcnt(0)\n"
    "v_lshlrev_b32 v10, 16, v64\n"
    "v_lshlrev_b32 v12, 16, v65\n"
    "v_lshlrev_b32 v14, 16, v66\n"
    "v_lshlrev_b32 v58, 16, v67\n"
    "v_and_b32 v11, s51, v64\n"
    "v_and_b32 v13, s51, v65\n"
    "v_and_b32 v15, s51, v66\n"
    "v_and_b32 v59, s51, v67\n"
    "v_pk_mul_f32 v[10:11], v[10:11], s[38:39]\n"
    "v_pk_mul_f32 v[12:13], v[12:13], s[38:39]\n"
    "v_pk_mul_f32 v[14:15], v[14:15], s[38:39]\n"
    "v_pk_mul_f32 v[58:59], v[58:59], s[38:39]\n"
    "v_cvt_pk_bf16_f32 v64, v10, v11\n"
    "v_cvt_pk_bf16_f32 v65, v12, v13\n"
    "v_cvt_pk_bf16_f32 v66, v14, v15\n"
    "v_cvt_pk_bf16_f32 v67, v58, v59\n"
    "v_lshlrev_b32 v10, 16, v68\n"
    "v_lshlrev_b32 v12, 16, v69\n"
    "v_lshlrev_b32 v14, 16, v70\n"
    "v_lshlrev_b32 v58, 16, v71\n"
    "v_and_b32 v11, s51, v68\n"
    "v_and_b32 v13, s51, v69\n"
    "v_and_b32 v15, s51, v70\n"
    "v_and_b32 v59, s51, v71\n"
    "v_pk_mul_f32 v[10:11], v[10:11], s[38:39]\n"
    "v_pk_mul_f32 v[12:13], v[12:13], s[38:39]\n"
    "v_pk_mul_f32 v[14:15], v[14:15], s[38:39]\n"
    "v_pk_mul_f32 v[58:59], v[58:59], s[38:39]\n"
    "v_cvt_pk_bf16_f32 v68, v10, v11\n"
    "v_cvt_pk_bf16_f32 v69, v12, v13\n"
    "v_cvt_pk_bf16_f32 v70, v14, v15\n"
    "v_cvt_pk_bf16_f32 v71, v58, v59\n"
    "v_lshlrev_b32 v10, 16, v72\n"
    "v_lshlrev_b32 v12, 16, v73\n"
    "v_lshlrev_b32 v14, 16, v74\n"
    "v_lshlrev_b32 v58, 16, v75\n"
    "v_and_b32 v11, s51, v72\n"
    "v_and_b32 v13, s51, v73\n"
    "v_and_b32 v15, s51, v74\n"
    "v_and_b32 v59, s51, v75\n"
    "v_pk_mul_f32 v[10:11], v[10:11], s[38:39]\n"
    "v_pk_mul_f32 v[12:13], v[12:13], s[38:39]\n"
    "v_pk_mul_f32 v[14:15], v[14:15], s[38:39]\n"
    "v_pk_mul_f32 v[58:59], v[58:59], s[38:39]\n"
    "v_cvt_pk_bf16_f32 v72, v10, v11\n"
    "v_cvt_pk_bf16_f32 v73, v12, v13\n"
    "v_cvt_pk_bf16_f32 v74, v14, v15\n"
    "v_cvt_pk_bf16_f32 v75, v58, v59\n"
    "v_lshlrev_b32 v10, 16, v76\n"
    "v_lshlrev_b32 v12, 16, v77\n"
    "v_lshlrev_b32 v14, 16, v78\n"
    "v_lshlrev_b32 v58, 16, v79\n"
    "v_and_b32 v11, s51, v76\n"
    "v_and_b32 v13, s51, v77\n"
    "v_and_b32 v15, s51, v78\n"
    "v_and_b32 v59, s51, v79\n"
    "v_pk_mul_f32 v[10:11], v[10:11], s[38:39]\n"
    "v_pk_mul_f32 v[12:13], v[12:13], s[38:39]\n"
    "v_pk_mul_f32 v[14:15], v[14:15], s[38:39]\n"
    "v_pk_mul_f32 v[58:59], v[58:59], s[38:39]\n"
    "v_cvt_pk_bf16_f32 v76, v10, v11\n"
    "v_cvt_pk_bf16_f32 v77, v12, v13\n"
    "v_cvt_pk_bf16_f32 v78, v14, v15\n"
    "v_cvt_pk_bf16_f32 v79, v58, v59\n"
    "s_waitcnt vmcnt(8)\n"
    "s_barrier\n"
    "ds_read_b128 a[0:3], v2\n"
    "ds_read_b128 a[4:7], v2 offset:256\n"
    "ds_read_b128 a[8:11], v2 offset:512\n"
    "ds_read_b128 a[12:15], v2 offset:768\n"
    "ds_read_b128 a[16:19], v2 offset:4096\n"
    "ds_read_b128 a[20:23], v2 offset:4352\n"
    "ds_read_b128 a[24:27], v2 offset:4608\n"
    "ds_read_b128 a[28:31], v2 offset:4864\n"
    "s_add_u32 m0, s43, 0x19000\n"
    "s_nop 0\n"
    "buffer_load_dwordx4 v5, s[28:31], s50 offen nt lds\n"
    "buffer_load_dwordx4 v7, s[28:31], s50 offen offset:1024 nt lds\n"
    "buffer_load_dwordx4 v8, s[28:31], s50 offen offset:2048 nt lds\n"
    "buffer_load_dwordx4 v6, s[28:31], s50 offen offset:3072 nt lds\n"
    "s_waitcnt lgkmcnt(0)\n"
    "v_mfma_f32_16x16x32_bf16 v[24:27], a[0:3], v[64:67], 0\n"
    "ds_read_b128 a[32:35], v2 offset:8192\n"
    "v_mfma_f32_16x16x32_bf16 v[24:27], a[4:7], v[68:71], v[24:27]\n"
    "ds_read_b128 a[36:39], v2 offset:8448\n"
    "v_mfma_f32_16x16x32_bf16 v[24:27], a[8:11], v[72:75], v[24:27]\n"
    "ds_read_b128 a[40:43], v2 offset:8704\n"
    "v_mfma_f32_16x16x32_bf16 v[24:27], a[12:15], v[76:79], v[24:27]\n"
    "ds_read_b128 a[44:47], v2 offset:8960\n"
    "v_mfma_f32_16x16x32_bf16 v[28:31], a[16:19], v[64:67], 0\n"
    "ds_read_b128 a[48:51], v2 offset:12288\n"
    "v_mfma_f32_16x16x32_bf16 v[28:31], a[20:23], v[68:71], v[28:31]\n"
    "ds_read_b128 a[52:55], v2 offset:12544\n"
    "v_mfma_f32_16x16x32_bf16 v[28:31], a[24:27], v[72:75], v[28:31]\n"
    "ds_read_b128 a[56:59], v2 offset:12800\n"
    "v_mfma_f32_16x16x32_bf16 v[28:31], a[28:31], v[76:79], v[28:31]\n"
    "ds_read_b128 a[60:63], v2 offset:13056\n"
    "s_waitcnt vmcnt(8)\n"
    "ds_read_b128 v[80:83], v56 offset:4096\n"
    "ds_read_b128 v[84:87], v56 offset:4352\n"
    "ds_read_b128 v[88:91], v56 offset:4608\n"
    "ds_read_b128 v[92:95], v56 offset:4864\n"
    "s_add_u32 m0, s37, 0x10000\n"
    "s_nop 0\n"
    "buffer_load_dwordx4 v5, s[24:27], s36 offen nt lds\n"
    "buffer_load_dwordx4 v7, s[24:27], s36 offen offset:1024 nt lds\n"
    "buffer_load_dwordx4 v5, s[24:27], s36 offen offset:2048 nt lds\n"
    "buffer_load_dwordx4 v7, s[24:27], s36 offen offset:3072 nt lds\n"
    "v_max3_f32 v60, v24, v25, v26\n"
    "v_max3_f32 v61, v27, v28, v29\n"
    "v_max3_f32 v60, v60, v30, v31\n"
    "v_max_f32 v60, v60, v61\n"
    "v_mov_b32 v61, v60\n"
    "s_nop 0\n"
    "s_nop 0\n"
    "v_permlane32_swap_b32 v60, v61\n"
    "v_max_f32 v60, v60, v61\n"
    "v_mov_b32 v61, v60\n"
    "s_nop 0\n"
    "s_nop 0\n"
    "v_permlane16_swap_b32 v60, v61\n"
    "v_max_f32 v60, v60, v61\n"
    "s_waitcnt lgkmcnt(0)\n"
    "v_lshlrev_b32 v10, 16, v80\n"
    "v_lshlrev_b32 v12, 16, v81\n"
    "v_lshlrev_b32 v14, 16, v82\n"
    "v_lshlrev_b32 v58, 16, v83\n"
    "v_and_b32 v11, s51, v80\n"
    "v_and_b32 v13, s51, v81\n"
    "v_and_b32 v15, s51, v82\n"
    "v_and_b32 v59, s51, v83\n"
    "v_pk_mul_f32 v[10:11], v[10:11], s[38:39]\n"
    "v_pk_mul_f32 v[12:13], v[12:13], s[38:39]\n"
    "v_pk_mul_f32 v[14:15], v[14:15], s[38:39]\n"
    "v_pk_mul_f32 v[58:59], v[58:59], s[38:39]\n"
    "v_cvt_pk_bf16_f32 v80, v10, v11\n"
    "v_cvt_pk_bf16_f32 v81, v12, v13\n"
    "v_cvt_pk_bf16_f32 v82, v14, v15\n"
    "v_cvt_pk_bf16_f32 v83, v58, v59\n"
    "v_lshlrev_b32 v10, 16, v84\n"
    "v_lshlrev_b32 v12, 16, v85\n"
    "v_lshlrev_b32 v14, 16, v86\n"
    "v_lshlrev_b32 v58, 16, v87\n"
    "v_and_b32 v11, s51, v84\n"
    "v_and_b32 v13, s51, v85\n"
    "v_and_b32 v15, s51, v86\n"
    "v_and_b32 v59, s51, v87\n"
    "v_pk_mul_f32 v[10:11], v[10:11], s[38:39]\n"
    "v_pk_mul_f32 v[12:13], v[12:13], s[38:39]\n"
    "v_pk_mul_f32 v[14:15], v[14:15], s[38:39]\n"
    "v_pk_mul_f32 v[58:59], v[58:59], s[38:39]\n"
    "v_cvt_pk_bf16_f32 v84, v10, v11\n"
    "v_cvt_pk_bf16_f32 v85, v12, v13\n"
    "v_cvt_pk_bf16_f32 v86, v14, v15\n"
    "v_cvt_pk_bf16_f32 v87, v58, v59\n"
    "v_lshlrev_b32 v10, 16, v88\n"
    "v_lshlrev_b32 v12, 16, v89\n"
    "v_lshlrev_b32 v14, 16, v90\n"
    "v_lshlrev_b32 v58, 16, v91\n"
    "v_and_b32 v11, s51, v88\n"
    "v_and_b32 v13, s51, v89\n"
    "v_and_b32 v15, s51, v90\n"
    "v_and_b32 v59, s51, v91\n"
    "v_pk_mul_f32 v[10:11], v[10:11], s[38:39]\n"
    "v_pk_mul_f32 v[12:13], v[12:13], s[38:39]\n"
    "v_pk_mul_f32 v[14:15], v[14:15], s[38:39]\n"
    "v_pk_mul_f32 v[58:59], v[58:59], s[38:39]\n"
    "v_cvt_pk_bf16_f32 v88, v10, v11\n"
    "v_cvt_pk_bf16_f32 v89, v12, v13\n"
    "v_cvt_pk_bf16_f32 v90, v14, v15\n"
    "v_cvt_pk_bf16_f32 v91, v58, v59\n"
    "v_lshlrev_b32 v10, 16, v92\n"
    "v_lshlrev_b32 v12, 16, v93\n"
    "v_lshlrev_b32 v14, 16, v94\n"
    "v_lshlrev_b32 v58, 16, v95\n"
    "v_and_b32 v11, s51, v92\n"
    "v_and_b32 v13, s51, v93\n"
    "v_and_b32 v15, s51, v94\n"
    "v_and_b32 v59, s51, v95\n"
    "v_pk_mul_f32 v[10:11], v[10:11], s[38:39]\n"
    "v_pk_mul_f32 v[12:13], v[12:13], s[38:39]\n"
    "v_pk_mul_f32 v[14:15], v[14:15], s[38:39]\n"
    "v_pk_mul_f32 v[58:59], v[58:59], s[38:39]\n"
    "v_cvt_pk_bf16_f32 v92, v10, v11\n"
    "v_cvt_pk_bf16_f32 v93, v12, v13\n"
    "v_cvt_pk_bf16_f32 v94, v14, v15\n"
    "v_cvt_pk_bf16_f32 v95, v58, v59\n"
    "v_mfma_f32_16x16x32_bf16 v[32:35], a[0:3], v[80:83], 0\n"
    "v_sub_f32 v24, v24, v60\n"
    "v_sub_f32 v25, v25, v60\n"
    "v_mfma_f32_16x16x32_bf16 v[32:35], a[4:7], v[84:87], v[32:35]\n"
    "v_sub_f32 v26, v26, v60\n"
    "v_sub_f32 v27, v27, v60\n"
    "v_mfma_f32_16x16x32_bf16 v[32:35], a[8:11], v[88:91], v[32:35]\n"
    "v_sub_f32 v28, v28, v60\n"
    "v_sub_f32 v29, v29, v60\n"
    "v_mfma_f32_16x16x32_bf16 v[32:35], a[12:15], v[92:95], v[32:35]\n"
    "v_sub_f32 v30, v30, v60\n"
    "v_sub_f32 v31, v31, v60\n"
    "v_mfma_f32_16x16x32_bf16 v[36:39], a[16:19], v[80:83], 0\n"
    "v_sub_f32 v40, 0, v60\n"
    "v_sub_f32 v41, 0, v60\n"
    "v_mfma_f32_16x16x32_bf16 v[36:39], a[20:23], v[84:87], v[36:39]\n"
    "v_sub_f32 v42, 0, v60\n"
    "v_sub_f32 v43, 0, v60\n"
    "v_mfma_f32_16x16x32_bf16 v[36:39], a[24:27], v[88:91], v[36:39]\n"
    "v_exp_f32_e32 v24, v24\n"
    "v_exp_f32_e32 v25, v25\n"
    "v_mfma_f32_16x16x32_bf16 v[36:39], a[28:31], v[92:95], v[36:39]\n"
    "v_exp_f32_e32 v26, v26\n"
    "v_cvt_pk_bf16_f32 v16, v24, v25\n"
    "v_exp_f32_e32 v27, v27\n"
    "v_exp_f32_e32 v28, v28\n"
    "v_cvt_pk_bf16_f32 v17, v26, v27\n"
    "v_exp_f32_e32 v29, v29\n"
    "v_exp_f32_e32 v30, v30\n"
    "v_cvt_pk_bf16_f32 v18, v28, v29\n"
    "v_exp_f32_e32 v31, v31\n"
    "s_nop 0\n"
    "v_cvt_pk_bf16_f32 v19, v30, v31\n"
    "s_waitcnt vmcnt(8)\n"
    "ds_read_b128 v[96:99], v57\n"
    "ds_read_b128 v[100:103], v57 offset:256\n"
    "ds_read_b128 v[104:107], v57 offset:512\n"
    "ds_read_b128 v[108:111], v57 offset:768\n"
    "s_add_u32 m0, s37, 0x4000\n"
    "s_mov_b32 s36, 0x4000\n"
    "buffer_load_dwordx4 v5, s[20:23], s36 offen nt lds\n"
    "buffer_load_dwordx4 v7, s[20:23], s36 offen offset:1024 nt lds\n"
    "buffer_load_dwordx4 v8, s[20:23], s36 offen offset:2048 nt lds\n"
    "buffer_load_dwordx4 v6, s[20:23], s36 offen offset:3072 nt lds\n"
    "v_max3_f32 v60, v32, v33, v34\n"
    "v_max3_f32 v61, v35, v36, v37\n"
    "v_max3_f32 v60, v60, v38, v39\n"
    "v_max_f32 v60, v60, v61\n"
    "v_mov_b32 v61, v60\n"
    "s_nop 0\n"
    "s_nop 0\n"
    "v_permlane32_swap_b32 v60, v61\n"
    "v_max_f32 v60, v60, v61\n"
    "v_mov_b32 v61, v60\n"
    "s_nop 0\n"
    "s_nop 0\n"
    "v_permlane16_swap_b32 v60, v61\n"
    "v_max_f32 v60, v60, v61\n"
    "s_waitcnt lgkmcnt(0)\n"
    "v_lshlrev_b32 v10, 16, v96\n"
    "v_lshlrev_b32 v12, 16, v97\n"
    "v_lshlrev_b32 v14, 16, v98\n"
    "v_lshlrev_b32 v58, 16, v99\n"
    "v_and_b32 v11, s51, v96\n"
    "v_and_b32 v13, s51, v97\n"
    "v_and_b32 v15, s51, v98\n"
    "v_and_b32 v59, s51, v99\n"
    "v_pk_mul_f32 v[10:11], v[10:11], s[38:39]\n"
    "v_pk_mul_f32 v[12:13], v[12:13], s[38:39]\n"
    "v_pk_mul_f32 v[14:15], v[14:15], s[38:39]\n"
    "v_pk_mul_f32 v[58:59], v[58:59], s[38:39]\n"
    "v_cvt_pk_bf16_f32 v96, v10, v11\n"
    "v_cvt_pk_bf16_f32 v97, v12, v13\n"
    "v_cvt_pk_bf16_f32 v98, v14, v15\n"
    "v_cvt_pk_bf16_f32 v99, v58, v59\n"
    "v_lshlrev_b32 v10, 16, v100\n"
    "v_lshlrev_b32 v12, 16, v101\n"
    "v_lshlrev_b32 v14, 16, v102\n"
    "v_lshlrev_b32 v58, 16, v103\n"
    "v_and_b32 v11, s51, v100\n"
    "v_and_b32 v13, s51, v101\n"
    "v_and_b32 v15, s51, v102\n"
    "v_and_b32 v59, s51, v103\n"
    "v_pk_mul_f32 v[10:11], v[10:11], s[38:39]\n"
    "v_pk_mul_f32 v[12:13], v[12:13], s[38:39]\n"
    "v_pk_mul_f32 v[14:15], v[14:15], s[38:39]\n"
    "v_pk_mul_f32 v[58:59], v[58:59], s[38:39]\n"
    "v_cvt_pk_bf16_f32 v100, v10, v11\n"
    "v_cvt_pk_bf16_f32 v101, v12, v13\n"
    "v_cvt_pk_bf16_f32 v102, v14, v15\n"
    "v_cvt_pk_bf16_f32 v103, v58, v59\n"
    "v_lshlrev_b32 v10, 16, v104\n"
    "v_lshlrev_b32 v12, 16, v105\n"
    "v_lshlrev_b32 v14, 16, v106\n"
    "v_lshlrev_b32 v58, 16, v107\n"
    "v_and_b32 v11, s51, v104\n"
    "v_and_b32 v13, s51, v105\n"
    "v_and_b32 v15, s51, v106\n"
    "v_and_b32 v59, s51, v107\n"
    "v_pk_mul_f32 v[10:11], v[10:11], s[38:39]\n"
    "v_pk_mul_f32 v[12:13], v[12:13], s[38:39]\n"
    "v_pk_mul_f32 v[14:15], v[14:15], s[38:39]\n"
    "v_pk_mul_f32 v[58:59], v[58:59], s[38:39]\n"
    "v_cvt_pk_bf16_f32 v104, v10, v11\n"
    "v_cvt_pk_bf16_f32 v105, v12, v13\n"
    "v_cvt_pk_bf16_f32 v106, v14, v15\n"
    "v_cvt_pk_bf16_f32 v107, v58, v59\n"
    "v_lshlrev_b32 v10, 16, v108\n"
    "v_lshlrev_b32 v12, 16, v109\n"
    "v_lshlrev_b32 v14, 16, v110\n"
    "v_lshlrev_b32 v58, 16, v111\n"
    "v_and_b32 v11, s51, v108\n"
    "v_and_b32 v13, s51, v109\n"
    "v_and_b32 v15, s51, v110\n"
    "v_and_b32 v59, s51, v111\n"
    "v_pk_mul_f32 v[10:11], v[10:11], s[38:39]\n"
    "v_pk_mul_f32 v[12:13], v[12:13], s[38:39]\n"
    "v_pk_mul_f32 v[14:15], v[14:15], s[38:39]\n"
    "v_pk_mul_f32 v[58:59], v[58:59], s[38:39]\n"
    "v_cvt_pk_bf16_f32 v108, v10, v11\n"
    "v_cvt_pk_bf16_f32 v109, v12, v13\n"
    "v_cvt_pk_bf16_f32 v110, v14, v15\n"
    "v_cvt_pk_bf16_f32 v111, v58, v59\n"
    "v_mfma_f32_16x16x32_bf16 v[24:27], a[0:3], v[96:99], 0\n"
    "v_sub_f32 v32, v32, v60\n"
    "v_sub_f32 v33, v33, v60\n"
    "v_mfma_f32_16x16x32_bf16 v[24:27], a[4:7], v[100:103], v[24:27]\n"
    "v_sub_f32 v34, v34, v60\n"
    "v_sub_f32 v35, v35, v60\n"
    "v_mfma_f32_16x16x32_bf16 v[24:27], a[8:11], v[104:107], v[24:27]\n"
    "v_sub_f32 v36, v36, v60\n"
    "v_sub_f32 v37, v37, v60\n"
    "v_mfma_f32_16x16x32_bf16 v[24:27], a[12:15], v[108:111], v[24:27]\n"
    "v_sub_f32 v38, v38, v60\n"
    "v_sub_f32 v39, v39, v60\n"
    "v_mfma_f32_16x16x32_bf16 v[28:31], a[16:19], v[96:99], 0\n"
    "v_sub_f32 v44, 0, v60\n"
    "v_sub_f32 v45, 0, v60\n"
    "v_mfma_f32_16x16x32_bf16 v[28:31], a[20:23], v[100:103], v[28:31]\n"
    "v_sub_f32 v46, 0, v60\n"
    "v_sub_f32 v47, 0, v60\n"
    "v_mfma_f32_16x16x32_bf16 v[28:31], a[24:27], v[104:107], v[28:31]\n"
    "v_exp_f32_e32 v32, v32\n"
    "v_exp_f32_e32 v33, v33\n"
    "v_mfma_f32_16x16x32_bf16 v[28:31], a[28:31], v[108:111], v[28:31]\n"
    "v_exp_f32_e32 v34, v34\n"
    "v_cvt_pk_bf16_f32 v20, v32, v33\n"
    "v_exp_f32_e32 v35, v35\n"
    "v_exp_f32_e32 v36, v36\n"
    "v_cvt_pk_bf16_f32 v21, v34, v35\n"
    "v_exp_f32_e32 v37, v37\n"
    "v_exp_f32_e32 v38, v38\n"
    "v_cvt_pk_bf16_f32 v22, v36, v37\n"
    "v_exp_f32_e32 v39, v39\n"
    "s_nop 0\n"
    "v_cvt_pk_bf16_f32 v23, v38, v39\n"
    "s_waitcnt vmcnt(8)\n"
    "ds_read_b128 v[112:115], v57 offset:4096\n"
    "ds_read_b128 v[116:119], v57 offset:4352\n"
    "ds_read_b128 v[120:123], v57 offset:4608\n"
    "ds_read_b128 v[124:127], v57 offset:4864\n"
    "s_add_u32 m0, s37, 0x14000\n"
    "s_nop 0\n"
    "buffer_load_dwordx4 v5, s[24:27], s36 offen nt lds\n"
    "buffer_load_dwordx4 v7, s[24:27], s36 offen offset:1024 nt lds\n"
    "buffer_load_dwordx4 v5, s[24:27], s36 offen offset:2048 nt lds\n"
    "buffer_load_dwordx4 v7, s[24:27], s36 offen offset:3072 nt lds\n"
    "v_max3_f32 v60, v24, v25, v26\n"
    "v_max3_f32 v61, v27, v28, v29\n"
    "v_max3_f32 v60, v60, v30, v31\n"
    "v_max_f32 v60, v60, v61\n"
    "v_mov_b32 v61, v60\n"
    "s_nop 0\n"
    "s_nop 0\n"
    "v_permlane32_swap_b32 v60, v61\n"
    "v_max_f32 v60, v60, v61\n"
    "v_mov_b32 v61, v60\n"
    "s_nop 0\n"
    "s_nop 0\n"
    "v_permlane16_swap_b32 v60, v61\n"
    "v_max_f32 v60, v60, v61\n"
    "s_waitcnt lgkmcnt(0)\n"
    "v_lshlrev_b32 v10, 16, v112\n"
    "v_lshlrev_b32 v12, 16, v113\n"
    "v_lshlrev_b32 v14, 16, v114\n"
    "v_lshlrev_b32 v58, 16, v115\n"
    "v_and_b32 v11, s51, v112\n"
    "v_and_b32 v13, s51, v113\n"
    "v_and_b32 v15, s51, v114\n"
    "v_and_b32 v59, s51, v115\n"
    "v_pk_mul_f32 v[10:11], v[10:11], s[38:39]\n"
    "v_pk_mul_f32 v[12:13], v[12:13], s[38:39]\n"
    "v_pk_mul_f32 v[14:15], v[14:15], s[38:39]\n"
    "v_pk_mul_f32 v[58:59], v[58:59], s[38:39]\n"
    "v_cvt_pk_bf16_f32 v112, v10, v11\n"
    "v_cvt_pk_bf16_f32 v113, v12, v13\n"
    "v_cvt_pk_bf16_f32 v114, v14, v15\n"
    "v_cvt_pk_bf16_f32 v115, v58, v59\n"
    "v_lshlrev_b32 v10, 16, v116\n"
    "v_lshlrev_b32 v12, 16, v117\n"
    "v_lshlrev_b32 v14, 16, v118\n"
    "v_lshlrev_b32 v58, 16, v119\n"
    "v_and_b32 v11, s51, v116\n"
    "v_and_b32 v13, s51, v117\n"
    "v_and_b32 v15, s51, v118\n"
    "v_and_b32 v59, s51, v119\n"
    "v_pk_mul_f32 v[10:11], v[10:11], s[38:39]\n"
    "v_pk_mul_f32 v[12:13], v[12:13], s[38:39]\n"
    "v_pk_mul_f32 v[14:15], v[14:15], s[38:39]\n"
    "v_pk_mul_f32 v[58:59], v[58:59], s[38:39]\n"
    "v_cvt_pk_bf16_f32 v116, v10, v11\n"
    "v_cvt_pk_bf16_f32 v117, v12, v13\n"
    "v_cvt_pk_bf16_f32 v118, v14, v15\n"
    "v_cvt_pk_bf16_f32 v119, v58, v59\n"
    "v_lshlrev_b32 v10, 16, v120\n"
    "v_lshlrev_b32 v12, 16, v121\n"
    "v_lshlrev_b32 v14, 16, v122\n"
    "v_lshlrev_b32 v58, 16, v123\n"
    "v_and_b32 v11, s51, v120\n"
    "v_and_b32 v13, s51, v121\n"
    "v_and_b32 v15, s51, v122\n"
    "v_and_b32 v59, s51, v123\n"
    "v_pk_mul_f32 v[10:11], v[10:11], s[38:39]\n"
    "v_pk_mul_f32 v[12:13], v[12:13], s[38:39]\n"
    "v_pk_mul_f32 v[14:15], v[14:15], s[38:39]\n"
    "v_pk_mul_f32 v[58:59], v[58:59], s[38:39]\n"
    "v_cvt_pk_bf16_f32 v120, v10, v11\n"
    "v_cvt_pk_bf16_f32 v121, v12, v13\n"
    "v_cvt_pk_bf16_f32 v122, v14, v15\n"
    "v_cvt_pk_bf16_f32 v123, v58, v59\n"
    "v_lshlrev_b32 v10, 16, v124\n"
    "v_lshlrev_b32 v12, 16, v125\n"
    "v_lshlrev_b32 v14, 16, v126\n"
    "v_lshlrev_b32 v58, 16, v127\n"
    "v_and_b32 v11, s51, v124\n"
    "v_and_b32 v13, s51, v125\n"
    "v_and_b32 v15, s51, v126\n"
    "v_and_b32 v59, s51, v127\n"
    "v_pk_mul_f32 v[10:11], v[10:11], s[38:39]\n"
    "v_pk_mul_f32 v[12:13], v[12:13], s[38:39]\n"
    "v_pk_mul_f32 v[14:15], v[14:15], s[38:39]\n"
    "v_pk_mul_f32 v[58:59], v[58:59], s[38:39]\n"
    "v_cvt_pk_bf16_f32 v124, v10, v11\n"
    "v_cvt_pk_bf16_f32 v125, v12, v13\n"
    "v_cvt_pk_bf16_f32 v126, v14, v15\n"
    "v_cvt_pk_bf16_f32 v127, v58, v59\n"
    "v_mfma_f32_16x16x32_bf16 v[32:35], a[0:3], v[112:115], 0\n"
    "v_sub_f32 v24, v24, v60\n"
    "v_sub_f32 v25, v25, v60\n"
    "v_mfma_f32_16x16x32_bf16 v[32:35], a[4:7], v[116:119], v[32:35]\n"
    "v_sub_f32 v26, v26, v60\n"
    "v_sub_f32 v27, v27, v60\n"
    "v_mfma_f32_16x16x32_bf16 v[32:35], a[8:11], v[120:123], v[32:35]\n"
    "v_sub_f32 v28, v28, v60\n"
    "v_sub_f32 v29, v29, v60\n"
    "v_mfma_f32_16x16x32_bf16 v[32:35], a[12:15], v[124:127], v[32:35]\n"
    "v_sub_f32 v30, v30, v60\n"
    "v_sub_f32 v31, v31, v60\n"
    "v_mfma_f32_16x16x32_bf16 v[36:39], a[16:19], v[112:115], 0\n"
    "v_sub_f32 v48, 0, v60\n"
    "v_sub_f32 v49, 0, v60\n"
    "v_mfma_f32_16x16x32_bf16 v[36:39], a[20:23], v[116:119], v[36:39]\n"
    "v_sub_f32 v50, 0, v60\n"
    "v_sub_f32 v51, 0, v60\n"
    "v_mfma_f32_16x16x32_bf16 v[36:39], a[24:27], v[120:123], v[36:39]\n"
    "v_mfma_f32_16x16x32_bf16 v[36:39], a[28:31], v[124:127], v[36:39]\n"
    "s_waitcnt vmcnt(8)\n"
    "s_memrealtime s[94:95]\n"
    "s_barrier\n"
    "ds_read_b64_tr_b16 a[64:65], v3\n"
    "ds_read_b64_tr_b16 a[66:67], v3 offset:4096\n"
    "v_max3_f32 v60, v32, v33, v34\n"
    "s_nop 1\n"
    "v_max3_f32 v61, v35, v36, v37\n"
    "v_max3_f32 v60, v60, v38, v39\n"
    "ds_read_b64_tr_b16 a[68:69], v4\n"
    "ds_read_b64_tr_b16 a[70:71], v4 offset:4096\n"
    "v_max_f32 v60, v60, v61\n"
    "v_mov_b32 v61, v60\n"
    "s_nop 0\n"
    "ds_read_b64_tr_b16 a[72:73], v3 offset:256\n"
    "ds_read_b64_tr_b16 a[74:75], v3 offset:4352\n"
    "v_permlane32_swap_b32 v60, v61\n"
    "v_max_f32 v60, v60, v61\n"
    "v_mov_b32 v61, v60\n"
    "ds_read_b64_tr_b16 a[76:77], v4 offset:256\n"
    "ds_read_b64_tr_b16 a[78:79], v4 offset:4352\n"
    "s_nop 0\n"
    "v_permlane16_swap_b32 v60, v61\n"
    "v_max_f32 v60, v60, v61\n"
    "ds_read_b64_tr_b16 a[80:81], v3 offset:512\n"
    "ds_read_b64_tr_b16 a[82:83], v3 offset:4608\n"
    "v_sub_f32 v32, v32, v60\n"
    "v_sub_f32 v33, v33, v60\n"
    "v_sub_f32 v34, v34, v60\n"
    "ds_read_b64_tr_b16 a[84:85], v4 offset:512\n"
    "ds_read_b64_tr_b16 a[86:87], v4 offset:4608\n"
    "v_sub_f32 v35, v35, v60\n"
    "v_sub_f32 v36, v36, v60\n"
    "v_sub_f32 v37, v37, v60\n"
    "ds_read_b64_tr_b16 a[88:89], v3 offset:768\n"
    "ds_read_b64_tr_b16 a[90:91], v3 offset:4864\n"
    "v_sub_f32 v38, v38, v60\n"
    "v_sub_f32 v39, v39, v60\n"
    "v_sub_f32 v52, 0, v60\n"
    "ds_read_b64_tr_b16 a[92:93], v4 offset:768\n"
    "ds_read_b64_tr_b16 a[94:95], v4 offset:4864\n"
    "v_sub_f32 v53, 0, v60\n"
    "v_sub_f32 v54, 0, v60\n"
    "v_sub_f32 v55, 0, v60\n"
    "s_xor_b32 s37, s37, 0xc000\n"
    "s_mov_b32 s36, 0x8000\n"
    "s_mov_b32 m0, s37\n"
    "s_nop 0\n"
    "buffer_load_dwordx4 v5, s[20:23], s36 offen nt lds\n"
    "buffer_load_dwordx4 v7, s[20:23], s36 offen offset:1024 nt lds\n"
    "buffer_load_dwordx4 v8, s[20:23], s36 offen offset:2048 nt lds\n"
    "buffer_load_dwordx4 v6, s[20:23], s36 offen offset:3072 nt lds\n"
    "s_waitcnt lgkmcnt(0)\n"
    "s_sub_u32 s94, s94, s18\n"
    "s_add_u32 s94, s94, 100\n"
    "s_min_u32 s94, s94, 500\n"
    "s_add_u32 s18, s18, s94\n"
    "v_mfma_f32_16x16x32_bf16 v[128:131], a[64:67], v[16:19], v[128:131]\n"
    "v_exp_f32_e32 v24, v24\n"
    "v_mfma_f32_16x16x32_bf16 v[132:135], a[68:71], v[16:19], v[132:135]\n"
    "ds_read_b64_tr_b16 a[96:97], v3 offset:8192\n"
    "v_exp_f32_e32 v25, v25\n"
    "v_mfma_f32_16x16x32_bf16 v[136:139], a[72:75], v[16:19], v[136:139]\n"
    "ds_read_b64_tr_b16 a[98:99], v3 offset:12288\n"
    "v_exp_f32_e32 v26, v26\n"
    "v_mfma_f32_16x16x32_bf16 v[140:143], a[76:79], v[16:19], v[140:143]\n"
    "ds_read_b64_tr_b16 a[100:101], v4 offset:8192\n"
    "v_exp_f32_e32 v27, v27\n"
    "v_mfma_f32_16x16x32_bf16 v[144:147], a[80:83], v[16:19], v[144:147]\n"
    "ds_read_b64_tr_b16 a[102:103], v4 offset:12288\n"
    "v_mfma_f32_16x16x32_bf16 v[148:151], a[84:87], v[16:19], v[148:151]\n"
    "v_exp_f32_e32 v28, v28\n"
    "ds_read_b64_tr_b16 a[104:105], v3 offset:8448\n"
    "v_mfma_f32_16x16x32_bf16 v[152:155], a[88:91], v[16:19], v[152:155]\n"
    "v_exp_f32_e32 v29, v29\n"
    "ds_read_b64_tr_b16 a[106:107], v3 offset:12544\n"
    "v_mfma_f32_16x16x32_bf16 v[156:159], a[92:95], v[16:19], v[156:159]\n"
    "v_exp_f32_e32 v30, v30\n"
    "ds_read_b64_tr_b16 a[108:109], v4 offset:8448\n"
    "v_mfma_f32_16x16x32_bf16 a[132:135], a[128:131], v[16:19], a[132:135]\n"
    "v_exp_f32_e32 v31, v31\n"
    "ds_read_b64_tr_b16 a[110:111], v4 offset:12544\n"
    "v_mfma_f32_16x16x32_bf16 v[160:163], a[64:67], v[20:23], v[160:163]\n"
    "v_mfma_f32_16x16x32_bf16 v[164:167], a[68:71], v[20:23], v[164:167]\n"
    "v_cvt_pk_bf16_f32 v16, v24, v25\n"
    "v_mfma_f32_16x16x32_bf16 v[168:171], a[72:75], v[20:23], v[168:171]\n"
    "v_cvt_pk_bf16_f32 v17, v26, v27\n"
    "v_mfma_f32_16x16x32_bf16 v[172:175], a[76:79], v[20:23], v[172:175]\n"
    "v_cvt_pk_bf16_f32 v18, v28, v29\n"
    "v_mfma_f32_16x16x32_bf16 v[176:179], a[80:83], v[20:23], v[176:179]\n"
    "v_cvt_pk_bf16_f32 v19, v30, v31\n"
    "v_mfma_f32_16x16x32_bf16 v[180:183], a[84:87], v[20:23], v[180:183]\n"
    "v_exp_f32_e32 v32, v32\n"
    "v_mfma_f32_16x16x32_bf16 v[184:187], a[88:91], v[20:23], v[184:187]\n"
    "v_exp_f32_e32 v33, v33\n"
    "v_mfma_f32_16x16x32_bf16 v[188:191], a[92:95], v[20:23], v[188:191]\n"
    "v_exp_f32_e32 v34, v34\n"
    "v_mfma_f32_16x16x32_bf16 a[136:139], a[128:131], v[20:23], a[136:139]\n"
    "v_exp_f32_e32 v35, v35\n"
    "v_mfma_f32_16x16x32_bf16 v[192:195], a[64:67], v[16:19], v[192:195]\n"
    "ds_read_b64_tr_b16 a[112:113], v3 offset:8704\n"
    "v_mfma_f32_16x16x32_bf16 v[196:199], a[68:71], v[16:19], v[196:199]\n"
    "ds_read_b64_tr_b16 a[114:115], v3 offset:12800\n"
    "ds_read_b64_tr_b16 a[116:117], v4 offset:8704\n"
    "v_mfma_f32_16x16x32_bf16 v[200:203], a[72:75], v[16:19], v[200:203]\n"
    "ds_read_b64_tr_b16 a[118:119], v4 offset:12800\n"
    "v_mfma_f32_16x16x32_bf16 v[204:207], a[76:79], v[16:19], v[204:207]\n"
    "ds_read_b64_tr_b16 a[120:121], v3 offset:8960\n"
    "ds_read_b64_tr_b16 a[122:123], v3 offset:13056\n"
    "v_mfma_f32_16x16x32_bf16 v[208:211], a[80:83], v[16:19], v[208:211]\n"
    "ds_read_b64_tr_b16 a[124:125], v4 offset:8960\n"
    "v_mfma_f32_16x16x32_bf16 v[212:215], a[84:87], v[16:19], v[212:215]\n"
    "ds_read_b64_tr_b16 a[126:127], v4 offset:13056\n"
    "v_xor_b32 v2, s35, v2\n"
    "v_mfma_f32_16x16x32_bf16 v[216:219], a[88:91], v[16:19], v[216:219]\n"
    "s_add_u32 m0, s37, 0x10000\n"
    "v_mfma_f32_16x16x32_bf16 v[220:223], a[92:95], v[16:19], v[220:223]\n"
    "v_cvt_pk_bf16_f32 v20, v32, v33\n"
    "v_cvt_pk_bf16_f32 v21, v34, v35\n"
    "s_waitcnt vmcnt(4)\n"
    "s_mov_b64 vcc, 7\n"
    "s_bfe_u32 s92, %[bid], 0x20003\n"
    "s_mul_i32 s92, s92, 28\n"
    "s_add_u32 s18, s18, s92\n"
    "s_add_u32 s18, s18, -152\n"
    ".Lsoft_%=:\n"
    "s_lshr_b64 vcc, vcc, 1\n"
    "s_add_u32 s18, s18, 152\n"
    ".Lsoftw_%=:\n"
    "s_memrealtime s[94:95]\n"
    "s_waitcnt lgkmcnt(0)\n"
    "s_sub_u32 s94, s94, s18\n"
    "s_cmp_lt_i32 s94, 0\n"
    "s_cbranch_scc1 .Lsoftw_%=\n"
    "s_sub_u32 s94, s94, 20\n"
    "s_max_i32 s94, s94, 0\n"
    "s_add_u32 s18, s18, s94\n"
    ".p2align 6\n"
    ".Lattn_loop_%=:\n"
    "v_mfma_f32_16x16x32_bf16 a[140:143], a[128:131], v[16:19], a[140:143]\n"
    "v_exp_f32_e32 v36, v36\n"
    "v_mfma_f32_16x16x32_bf16 v[24:27], a[32:35], v[64:67], v[40:43]\n"
    "s_barrier\n"
    "v_mfma_f32_16x16x32_bf16 v[24:27], a[36:39], v[68:71], v[24:27]\n"
    "v_exp_f32_e32 v37, v37\n"
    "v_mfma_f32_16x16x32_bf16 v[24:27], a[40:43], v[72:75], v[24:27]\n"
    "ds_read_b128 a[0:3], v2\n"
    "v_cvt_pk_bf16_f32 v22, v36, v37\n"
    "v_mfma_f32_16x16x32_bf16 v[24:27], a[44:47], v[76:79], v[24:27]\n"
    "v_exp_f32_e32 v38, v38\n"
    "v_mfma_f32_16x16x32_bf16 v[28:31], a[48:51], v[64:67], v[40:43]\n"
    "buffer_load_dwordx4 v5, s[24:27], s36 offen nt lds\n"
    "v_mfma_f32_16x16x32_bf16 v[28:31], a[52:55], v[68:71], v[28:31]\n"
    "v_exp_f32_e32 v39, v39\n"
    "v_mfma_f32_16x16x32_bf16 v[28:31], a[56:59], v[72:75], v[28:31]\n"
    "ds_read_b128 a[4:7], v2 offset:256\n"
    "v_cvt_pk_bf16_f32 v23, v38, v39\n"
    "v_mfma_f32_16x16x32_bf16 v[28:31], a[60:63], v[76:79], v[28:31]\n"
    "ds_read_b128 a[8:11], v2 offset:512\n"
    "v_xor_b32 v3, s35, v3\n"
    "v_mfma_f32_16x16x32_bf16 v[224:227], a[64:67], v[20:23], v[224:227]\n"
    "v_exp_f32_e32 v24, v24\n"
    "v_mfma_f32_16x16x32_bf16 v[228:231], a[68:71], v[20:23], v[228:231]\n"
    "ds_read_b128 a[12:15], v2 offset:768\n"
    "v_xor_b32 v4, s35, v4\n"
    "v_mfma_f32_16x16x32_bf16 v[232:235], a[72:75], v[20:23], v[232:235]\n"
    "v_exp_f32_e32 v25, v25\n"
    "v_mfma_f32_16x16x32_bf16 v[236:239], a[76:79], v[20:23], v[236:239]\n"
    "v_cvt_pk_bf16_f32 v16, v24, v25\n"
    "v_mfma_f32_16x16x32_bf16 v[240:243], a[80:83], v[20:23], v[240:243]\n"
    "v_exp_f32_e32 v26, v26\n"
    "v_mfma_f32_16x16x32_bf16 v[244:247], a[84:87], v[20:23], v[244:247]\n"
    "buffer_load_dwordx4 v7, s[24:27], s36 offen offset:1024 nt lds\n"
    "v_mfma_f32_16x16x32_bf16 v[248:251], a[88:91], v[20:23], v[248:251]\n"
    "v_exp_f32_e32 v27, v27\n"
    "v_mfma_f32_16x16x32_bf16 v[252:255], a[92:95], v[20:23], v[252:255]\n"
    "s_xor_b32 s37, s37, s35\n"
    "v_cvt_pk_bf16_f32 v17, v26, v27\n"
    "v_mfma_f32_16x16x32_bf16 a[144:147], a[128:131], v[20:23], a[144:147]\n"
    "v_exp_f32_e32 v28, v28\n"
    "v_mfma_f32_16x16x32_bf16 v[32:35], a[32:35], v[80:83], v[44:47]\n"
    "ds_read_b128 a[16:19], v2 offset:4096\n"
    "v_mfma_f32_16x16x32_bf16 v[32:35], a[36:39], v[84:87], v[32:35]\n"
    "v_exp_f32_e32 v29, v29\n"
    "v_mfma_f32_16x16x32_bf16 v[32:35], a[40:43], v[88:91], v[32:35]\n"
    "ds_read_b128 a[20:23], v2 offset:4352\n"
    "v_cvt_pk_bf16_f32 v18, v28, v29\n"
    "v_mfma_f32_16x16x32_bf16 v[32:35], a[44:47], v[92:95], v[32:35]\n"
    "v_exp_f32_e32 v30, v30\n"
    "v_mfma_f32_16x16x32_bf16 v[36:39], a[48:51], v[80:83], v[44:47]\n"
    "buffer_load_dwordx4 v5, s[24:27], s36 offen offset:2048 nt lds\n"
    "v_mfma_f32_16x16x32_bf16 v[36:39], a[52:55], v[84:87], v[36:39]\n"
    "v_exp_f32_e32 v31, v31\n"
    "v_mfma_f32_16x16x32_bf16 v[36:39], a[56:59], v[88:91], v[36:39]\n"
    "ds_read_b128 a[24:27], v2 offset:4608\n"
    "v_cvt_pk_bf16_f32 v19, v30, v31\n"
    "v_mfma_f32_16x16x32_bf16 v[36:39], a[60:63], v[92:95], v[36:39]\n"
    "ds_read_b128 a[28:31], v2 offset:4864\n"
    "s_waitcnt lgkmcnt(8)\n"
    "v_mfma_f32_16x16x32_bf16 v[128:131], a[96:99], v[16:19], v[128:131]\n"
    "v_exp_f32_e32 v32, v32\n"
    "v_mfma_f32_16x16x32_bf16 v[132:135], a[100:103], v[16:19], v[132:135]\n"
    "ds_read_b64_tr_b16 a[64:65], v3\n"
    "s_xor_b32 s35, s35, 0xc000\n"
    "v_mfma_f32_16x16x32_bf16 v[136:139], a[104:107], v[16:19], v[136:139]\n"
    "v_exp_f32_e32 v33, v33\n"
    "v_mfma_f32_16x16x32_bf16 v[140:143], a[108:111], v[16:19], v[140:143]\n"
    "ds_read_b64_tr_b16 a[66:67], v3 offset:4096\n"
    "v_cvt_pk_bf16_f32 v20, v32, v33\n"
    "v_mfma_f32_16x16x32_bf16 v[144:147], a[112:115], v[16:19], v[144:147]\n"
    "v_exp_f32_e32 v34, v34\n"
    "v_mfma_f32_16x16x32_bf16 v[148:151], a[116:119], v[16:19], v[148:151]\n"
    "buffer_load_dwordx4 v7, s[24:27], s36 offen offset:3072 nt lds\n"
    "v_mfma_f32_16x16x32_bf16 v[152:155], a[120:123], v[16:19], v[152:155]\n"
    "v_exp_f32_e32 v35, v35\n"
    "v_mfma_f32_16x16x32_bf16 v[156:159], a[124:127], v[16:19], v[156:159]\n"
    "v_cvt_pk_bf16_f32 v21, v34, v35\n"
    "v_mfma_f32_16x16x32_bf16 a[132:135], a[128:131], v[16:19], a[132:135]\n"
    "v_exp_f32_e32 v36, v36\n"
    "v_mfma_f32_16x16x32_bf16 v[24:27], a[32:35], v[96:99], v[48:51]\n"
    "ds_read_b64_tr_b16 a[68:69], v4\n"
    "v_mfma_f32_16x16x32_bf16 v[24:27], a[36:39], v[100:103], v[24:27]\n"
    "v_exp_f32_e32 v37, v37\n"
    "v_mfma_f32_16x16x32_bf16 v[24:27], a[40:43], v[104:107], v[24:27]\n"
    "ds_read_b64_tr_b16 a[70:71], v4 offset:4096\n"
    "v_cvt_pk_bf16_f32 v22, v36, v37\n"
    "v_mfma_f32_16x16x32_bf16 v[24:27], a[44:47], v[108:111], v[24:27]\n"
    "v_exp_f32_e32 v38, v38\n"
    "v_mfma_f32_16x16x32_bf16 v[28:31], a[48:51], v[96:99], v[48:51]\n"
    "ds_read_b64_tr_b16 a[72:73], v3 offset:256\n"
    "v_mfma_f32_16x16x32_bf16 v[28:31], a[52:55], v[100:103], v[28:31]\n"
    "v_exp_f32_e32 v39, v39\n"
    "v_mfma_f32_16x16x32_bf16 v[28:31], a[56:59], v[104:107], v[28:31]\n"
    "ds_read_b64_tr_b16 a[74:75], v3 offset:4352\n"
    "v_cvt_pk_bf16_f32 v23, v38, v39\n"
    "v_mfma_f32_16x16x32_bf16 v[28:31], a[60:63], v[108:111], v[28:31]\n"
    "ds_read_b64_tr_b16 a[76:77], v4 offset:256\n"
    "v_mfma_f32_16x16x32_bf16 v[160:163], a[96:99], v[20:23], v[160:163]\n"
    "v_exp_f32_e32 v24, v24\n"
    "v_mfma_f32_16x16x32_bf16 v[164:167], a[100:103], v[20:23], v[164:167]\n"
    "ds_read_b64_tr_b16 a[78:79], v4 offset:4352\n"
    "v_mfma_f32_16x16x32_bf16 v[168:171], a[104:107], v[20:23], v[168:171]\n"
    "v_exp_f32_e32 v25, v25\n"
    "v_mfma_f32_16x16x32_bf16 v[172:175], a[108:111], v[20:23], v[172:175]\n"
    "v_cvt_pk_bf16_f32 v16, v24, v25\n"
    "v_mfma_f32_16x16x32_bf16 v[176:179], a[112:115], v[20:23], v[176:179]\n"
    "v_exp_f32_e32 v26, v26\n"
    "v_mfma_f32_16x16x32_bf16 v[180:183], a[116:119], v[20:23], v[180:183]\n"
    "ds_read_b64_tr_b16 a[80:81], v3 offset:512\n"
    "v_mfma_f32_16x16x32_bf16 v[184:187], a[120:123], v[20:23], v[184:187]\n"
    "v_exp_f32_e32 v27, v27\n"
    "v_mfma_f32_16x16x32_bf16 v[188:191], a[124:127], v[20:23], v[188:191]\n"
    "v_cvt_pk_bf16_f32 v17, v26, v27\n"
    "v_mfma_f32_16x16x32_bf16 a[136:139], a[128:131], v[20:23], a[136:139]\n"
    "v_exp_f32_e32 v28, v28\n"
    "v_mfma_f32_16x16x32_bf16 v[32:35], a[32:35], v[112:115], v[52:55]\n"
    "ds_read_b64_tr_b16 a[82:83], v3 offset:4608\n"
    "s_waitcnt lgkmcnt(10)\n"
    "v_mfma_f32_16x16x32_bf16 v[32:35], a[36:39], v[116:119], v[32:35]\n"
    "v_exp_f32_e32 v29, v29\n"
    "v_mfma_f32_16x16x32_bf16 v[32:35], a[40:43], v[120:123], v[32:35]\n"
    "ds_read_b64_tr_b16 a[84:85], v4 offset:512\n"
    "v_cvt_pk_bf16_f32 v18, v28, v29\n"
    "v_mfma_f32_16x16x32_bf16 v[32:35], a[44:47], v[124:127], v[32:35]\n"
    "v_exp_f32_e32 v30, v30\n"
    "v_mfma_f32_16x16x32_bf16 v[36:39], a[48:51], v[112:115], v[52:55]\n"
    "ds_read_b64_tr_b16 a[86:87], v4 offset:4608\n"
    "v_mfma_f32_16x16x32_bf16 v[36:39], a[52:55], v[116:119], v[36:39]\n"
    "v_exp_f32_e32 v31, v31\n"
    "v_mfma_f32_16x16x32_bf16 v[36:39], a[56:59], v[120:123], v[36:39]\n"
    "ds_read_b64_tr_b16 a[88:89], v3 offset:768\n"
    "v_cvt_pk_bf16_f32 v19, v30, v31\n"
    "v_mfma_f32_16x16x32_bf16 v[36:39], a[60:63], v[124:127], v[36:39]\n"
    "ds_read_b64_tr_b16 a[90:91], v3 offset:4864\n"
    "s_add_u32 s36, s36, 0x4000\n"
    "v_mfma_f32_16x16x32_bf16 v[192:195], a[96:99], v[16:19], v[192:195]\n"
    "v_exp_f32_e32 v32, v32\n"
    "v_mfma_f32_16x16x32_bf16 v[196:199], a[100:103], v[16:19], v[196:199]\n"
    "ds_read_b64_tr_b16 a[92:93], v4 offset:768\n"
    "s_mov_b32 m0, s37\n"
    "v_mfma_f32_16x16x32_bf16 v[200:203], a[104:107], v[16:19], v[200:203]\n"
    "v_exp_f32_e32 v33, v33\n"
    "v_mfma_f32_16x16x32_bf16 v[204:207], a[108:111], v[16:19], v[204:207]\n"
    "v_cvt_pk_bf16_f32 v20, v32, v33\n"
    "v_mfma_f32_16x16x32_bf16 v[208:211], a[112:115], v[16:19], v[208:211]\n"
    "v_exp_f32_e32 v34, v34\n"
    "v_mfma_f32_16x16x32_bf16 v[212:215], a[116:119], v[16:19], v[212:215]\n"
    "ds_read_b64_tr_b16 a[94:95], v4 offset:4864\n"
    "s_cmp_eq_u32 s36, 0x44000\n"
    "v_mfma_f32_16x16x32_bf16 v[216:219], a[120:123], v[16:19], v[216:219]\n"
    "v_exp_f32_e32 v35, v35\n"
    "v_mfma_f32_16x16x32_bf16 v[220:223], a[124:127], v[16:19], v[220:223]\n"
    "v_cvt_pk_bf16_f32 v21, v34, v35\n"
    "s_cbranch_scc1 .Lattn_exit_%=\n"
    "v_mfma_f32_16x16x32_bf16 a[140:143], a[128:131], v[16:19], a[140:143]\n"
    "v_exp_f32_e32 v36, v36\n"
    "v_mfma_f32_16x16x32_bf16 v[24:27], a[0:3], v[64:67], v[40:43]\n"
    "ds_read_b128 a[32:35], v2 offset:8192\n"
    "v_mfma_f32_16x16x32_bf16 v[24:27], a[4:7], v[68:71], v[24:27]\n"
    "v_exp_f32_e32 v37, v37\n"
    "v_mfma_f32_16x16x32_bf16 v[24:27], a[8:11], v[72:75], v[24:27]\n"
    "ds_read_b128 a[36:39], v2 offset:8448\n"
    "v_cvt_pk_bf16_f32 v22, v36, v37\n"
    "v_mfma_f32_16x16x32_bf16 v[24:27], a[12:15], v[76:79], v[24:27]\n"
    "v_exp_f32_e32 v38, v38\n"
    "v_mfma_f32_16x16x32_bf16 v[28:31], a[16:19], v[64:67], v[40:43]\n"
    "buffer_load_dwordx4 v5, s[20:23], s36 offen nt lds\n"
    "v_mfma_f32_16x16x32_bf16 v[28:31], a[20:23], v[68:71], v[28:31]\n"
    "v_exp_f32_e32 v39, v39\n"
    "v_mfma_f32_16x16x32_bf16 v[28:31], a[24:27], v[72:75], v[28:31]\n"
    "ds_read_b128 a[40:43], v2 offset:8704\n"
    "v_cvt_pk_bf16_f32 v23, v38, v39\n"
    "v_mfma_f32_16x16x32_bf16 v[28:31], a[28:31], v[76:79], v[28:31]\n"
    "ds_read_b128 a[44:47], v2 offset:8960\n"
    "v_mfma_f32_16x16x32_bf16 v[224:227], a[96:99], v[20:23], v[224:227]\n"
    "v_exp_f32_e32 v24, v24\n"
    "v_mfma_f32_16x16x32_bf16 v[228:231], a[100:103], v[20:23], v[228:231]\n"
    "ds_read_b128 a[48:51], v2 offset:12288\n"
    "v_mfma_f32_16x16x32_bf16 v[232:235], a[104:107], v[20:23], v[232:235]\n"
    "v_exp_f32_e32 v25, v25\n"
    "v_mfma_f32_16x16x32_bf16 v[236:239], a[108:111], v[20:23], v[236:239]\n"
    "v_cvt_pk_bf16_f32 v16, v24, v25\n"
    "v_mfma_f32_16x16x32_bf16 v[240:243], a[112:115], v[20:23], v[240:243]\n"
    "v_exp_f32_e32 v26, v26\n"
    "v_mfma_f32_16x16x32_bf16 v[244:247], a[116:119], v[20:23], v[244:247]\n"
    "ds_read_b128 a[52:55], v2 offset:12544\n"
    "v_mfma_f32_16x16x32_bf16 v[248:251], a[120:123], v[20:23], v[248:251]\n"
    "v_exp_f32_e32 v27, v27\n"
    "v_mfma_f32_16x16x32_bf16 v[252:255], a[124:127], v[20:23], v[252:255]\n"
    "v_cvt_pk_bf16_f32 v17, v26, v27\n"
    "v_mfma_f32_16x16x32_bf16 a[144:147], a[128:131], v[20:23], a[144:147]\n"
    "v_exp_f32_e32 v28, v28\n"
    "v_mfma_f32_16x16x32_bf16 v[32:35], a[0:3], v[80:83], v[44:47]\n"
    "ds_read_b128 a[56:59], v2 offset:12800\n"
    "v_mfma_f32_16x16x32_bf16 v[32:35], a[4:7], v[84:87], v[32:35]\n"
    "v_exp_f32_e32 v29, v29\n"
    "v_mfma_f32_16x16x32_bf16 v[32:35], a[8:11], v[88:91], v[32:35]\n"
    "ds_read_b128 a[60:63], v2 offset:13056\n"
    "v_cvt_pk_bf16_f32 v18, v28, v29\n"
    "v_mfma_f32_16x16x32_bf16 v[32:35], a[12:15], v[92:95], v[32:35]\n"
    "v_exp_f32_e32 v30, v30\n"
    "v_mfma_f32_16x16x32_bf16 v[36:39], a[16:19], v[80:83], v[44:47]\n"
    "buffer_load_dwordx4 v7, s[20:23], s36 offen offset:1024 nt lds\n"
    "v_mfma_f32_16x16x32_bf16 v[36:39], a[20:23], v[84:87], v[36:39]\n"
    "v_exp_f32_e32 v31, v31\n"
    "v_mfma_f32_16x16x32_bf16 v[36:39], a[24:27], v[88:91], v[36:39]\n"
    "ds_read_b64_tr_b16 a[96:97], v3 offset:8192\n"
    "v_cvt_pk_bf16_f32 v19, v30, v31\n"
    "v_mfma_f32_16x16x32_bf16 v[36:39], a[28:31], v[92:95], v[36:39]\n"
    "ds_read_b64_tr_b16 a[98:99], v3 offset:12288\n"
    "s_waitcnt lgkmcnt(10)\n"
    "v_mfma_f32_16x16x32_bf16 v[128:131], a[64:67], v[16:19], v[128:131]\n"
    "v_exp_f32_e32 v32, v32\n"
    "v_mfma_f32_16x16x32_bf16 v[132:135], a[68:71], v[16:19], v[132:135]\n"
    "ds_read_b64_tr_b16 a[100:101], v4 offset:8192\n"
    "v_mfma_f32_16x16x32_bf16 v[136:139], a[72:75], v[16:19], v[136:139]\n"
    "v_exp_f32_e32 v33, v33\n"
    "v_mfma_f32_16x16x32_bf16 v[140:143], a[76:79], v[16:19], v[140:143]\n"
    "v_cvt_pk_bf16_f32 v20, v32, v33\n"
    "v_mfma_f32_16x16x32_bf16 v[144:147], a[80:83], v[16:19], v[144:147]\n"
    "v_exp_f32_e32 v34, v34\n"
    "v_mfma_f32_16x16x32_bf16 v[148:151], a[84:87], v[16:19], v[148:151]\n"
    "ds_read_b64_tr_b16 a[102:103], v4 offset:12288\n"
    "v_mfma_f32_16x16x32_bf16 v[152:155], a[88:91], v[16:19], v[152:155]\n"
    "v_exp_f32_e32 v35, v35\n"
    "v_mfma_f32_16x16x32_bf16 v[156:159], a[92:95], v[16:19], v[156:159]\n"
    "v_cvt_pk_bf16_f32 v21, v34, v35\n"
    "v_mfma_f32_16x16x32_bf16 a[132:135], a[128:131], v[16:19], a[132:135]\n"
    "v_exp_f32_e32 v36, v36\n"
    "v_mfma_f32_16x16x32_bf16 v[24:27], a[0:3], v[96:99], v[48:51]\n"
    "ds_read_b64_tr_b16 a[104:105], v3 offset:8448\n"
    "v_mfma_f32_16x16x32_bf16 v[24:27], a[4:7], v[100:103], v[24:27]\n"
    "v_exp_f32_e32 v37, v37\n"
    "v_mfma_f32_16x16x32_bf16 v[24:27], a[8:11], v[104:107], v[24:27]\n"
    "ds_read_b64_tr_b16 a[106:107], v3 offset:12544\n"
    "v_cvt_pk_bf16_f32 v22, v36, v37\n"
    "v_mfma_f32_16x16x32_bf16 v[24:27], a[12:15], v[108:111], v[24:27]\n"
    "v_exp_f32_e32 v38, v38\n"
    "v_mfma_f32_16x16x32_bf16 v[28:31], a[16:19], v[96:99], v[48:51]\n"
    "buffer_load_dwordx4 v8, s[20:23], s36 offen offset:2048 nt lds\n"
    "v_mfma_f32_16x16x32_bf16 v[28:31], a[20:23], v[100:103], v[28:31]\n"
    "v_exp_f32_e32 v39, v39\n"
    "v_mfma_f32_16x16x32_bf16 v[28:31], a[24:27], v[104:107], v[28:31]\n"
    "ds_read_b64_tr_b16 a[108:109], v4 offset:8448\n"
    "v_cvt_pk_bf16_f32 v23, v38, v39\n"
    "v_mfma_f32_16x16x32_bf16 v[28:31], a[28:31], v[108:111], v[28:31]\n"
    "ds_read_b64_tr_b16 a[110:111], v4 offset:12544\n"
    "v_mfma_f32_16x16x32_bf16 v[160:163], a[64:67], v[20:23], v[160:163]\n"
    "v_exp_f32_e32 v24, v24\n"
    "v_mfma_f32_16x16x32_bf16 v[164:167], a[68:71], v[20:23], v[164:167]\n"
    "ds_read_b64_tr_b16 a[112:113], v3 offset:8704\n"
    "v_xor_b32 v2, s35, v2\n"
    "v_mfma_f32_16x16x32_bf16 v[168:171], a[72:75], v[20:23], v[168:171]\n"
    "v_exp_f32_e32 v25, v25\n"
    "v_mfma_f32_16x16x32_bf16 v[172:175], a[76:79], v[20:23], v[172:175]\n"
    "v_cvt_pk_bf16_f32 v16, v24, v25\n"
    "v_mfma_f32_16x16x32_bf16 v[176:179], a[80:83], v[20:23], v[176:179]\n"
    "v_exp_f32_e32 v26, v26\n"
    "v_mfma_f32_16x16x32_bf16 v[180:183], a[84:87], v[20:23], v[180:183]\n"
    "ds_read_b64_tr_b16 a[114:115], v3 offset:12800\n"
    "v_mfma_f32_16x16x32_bf16 v[184:187], a[88:91], v[20:23], v[184:187]\n"
    "v_exp_f32_e32 v27, v27\n"
    "v_mfma_f32_16x16x32_bf16 v[188:191], a[92:95], v[20:23], v[188:191]\n"
    "v_cvt_pk_bf16_f32 v17, v26, v27\n"
    "v_mfma_f32_16x16x32_bf16 a[136:139], a[128:131], v[20:23], a[136:139]\n"
    "v_exp_f32_e32 v28, v28\n"
    "v_mfma_f32_16x16x32_bf16 v[32:35], a[0:3], v[112:115], v[52:55]\n"
    "ds_read_b64_tr_b16 a[116:117], v4 offset:8704\n"
    "s_waitcnt lgkmcnt(11)\n"
    "v_mfma_f32_16x16x32_bf16 v[32:35], a[4:7], v[116:119], v[32:35]\n"
    "v_exp_f32_e32 v29, v29\n"
    "v_mfma_f32_16x16x32_bf16 v[32:35], a[8:11], v[120:123], v[32:35]\n"
    "ds_read_b64_tr_b16 a[118:119], v4 offset:12800\n"
    "v_cvt_pk_bf16_f32 v18, v28, v29\n"
    "v_mfma_f32_16x16x32_bf16 v[32:35], a[12:15], v[124:127], v[32:35]\n"
    "v_exp_f32_e32 v30, v30\n"
    "v_mfma_f32_16x16x32_bf16 v[36:39], a[16:19], v[112:115], v[52:55]\n"
    "buffer_load_dwordx4 v6, s[20:23], s36 offen offset:3072 nt lds\n"
    "v_mfma_f32_16x16x32_bf16 v[36:39], a[20:23], v[116:119], v[36:39]\n"
    "v_exp_f32_e32 v31, v31\n"
    "v_mfma_f32_16x16x32_bf16 v[36:39], a[24:27], v[120:123], v[36:39]\n"
    "ds_read_b64_tr_b16 a[120:121], v3 offset:8960\n"
    "v_cvt_pk_bf16_f32 v19, v30, v31\n"
    "v_mfma_f32_16x16x32_bf16 v[36:39], a[28:31], v[124:127], v[36:39]\n"
    "ds_read_b64_tr_b16 a[122:123], v3 offset:13056\n"
    "v_mfma_f32_16x16x32_bf16 v[192:195], a[64:67], v[16:19], v[192:195]\n"
    "v_exp_f32_e32 v32, v32\n"
    "v_mfma_f32_16x16x32_bf16 v[196:199], a[68:71], v[16:19], v[196:199]\n"
    "ds_read_b64_tr_b16 a[124:125], v4 offset:8960\n"
    "s_add_u32 m0, s37, 0x10000\n"
    "v_mfma_f32_16x16x32_bf16 v[200:203], a[72:75], v[16:19], v[200:203]\n"
    "v_exp_f32_e32 v33, v33\n"
    "v_mfma_f32_16x16x32_bf16 v[204:207], a[76:79], v[16:19], v[204:207]\n"
    "s_waitcnt vmcnt(4)\n"
    "v_cvt_pk_bf16_f32 v20, v32, v33\n"
    "v_mfma_f32_16x16x32_bf16 v[208:211], a[80:83], v[16:19], v[208:211]\n"
    "v_exp_f32_e32 v34, v34\n"
    "v_mfma_f32_16x16x32_bf16 v[212:215], a[84:87], v[16:19], v[212:215]\n"
    "ds_read_b64_tr_b16 a[126:127], v4 offset:13056\n"
    "v_mfma_f32_16x16x32_bf16 v[216:219], a[88:91], v[16:19], v[216:219]\n"
    "v_exp_f32_e32 v35, v35\n"
    "v_mfma_f32_16x16x32_bf16 v[220:223], a[92:95], v[16:19], v[220:223]\n"
    "v_cvt_pk_bf16_f32 v21, v34, v35\n"
    "s_cbranch_vccnz .Lsoft_%=\n"
    "s_branch .Lattn_loop_%=\n"
    ".Lattn_exit_%=:\n"
    "v_mfma_f32_16x16x32_bf16 a[140:143], a[128:131], v[16:19], a[140:143]\n"
    "v_exp_f32_e32 v36, v36\n"
    "v_mfma_f32_16x16x32_bf16 v[24:27], a[0:3], v[64:67], v[40:43]\n"
    "ds_read_b128 a[32:35], v2 offset:8192\n"
    "ds_read_b128 a[36:39], v2 offset:8448\n"
    "v_mfma_f32_16x16x32_bf16 v[24:27], a[4:7], v[68:71], v[24:27]\n"
    "v_exp_f32_e32 v37, v37\n"
    "v_mfma_f32_16x16x32_bf16 v[24:27], a[8:11], v[72:75], v[24:27]\n"
    "ds_read_b128 a[40:43], v2 offset:8704\n"
    "v_cvt_pk_bf16_f32 v22, v36, v37\n"
    "v_mfma_f32_16x16x32_bf16 v[24:27], a[12:15], v[76:79], v[24:27]\n"
    "v_exp_f32_e32 v38, v38\n"
    "v_mfma_f32_16x16x32_bf16 v[28:31], a[16:19], v[64:67], v[40:43]\n"
    "ds_read_b128 a[44:47], v2 offset:8960\n"
    "ds_read_b128 a[48:51], v2 offset:12288\n"
    "v_mfma_f32_16x16x32_bf16 v[28:31], a[20:23], v[68:71], v[28:31]\n"
    "v_exp_f32_e32 v39, v39\n"
    "v_mfma_f32_16x16x32_bf16 v[28:31], a[24:27], v[72:75], v[28:31]\n"
    "ds_read_b128 a[52:55], v2 offset:12544\n"
    "v_cvt_pk_bf16_f32 v23, v38, v39\n"
    "v_mfma_f32_16x16x32_bf16 v[28:31], a[28:31], v[76:79], v[28:31]\n"
    "ds_read_b128 a[56:59], v2 offset:12800\n"
    "ds_read_b128 a[60:63], v2 offset:13056\n"
    "v_mfma_f32_16x16x32_bf16 v[224:227], a[96:99], v[20:23], v[224:227]\n"
    "v_exp_f32_e32 v24, v24\n"
    "v_mfma_f32_16x16x32_bf16 v[228:231], a[100:103], v[20:23], v[228:231]\n"
    "ds_read_b64_tr_b16 a[96:97], v3 offset:8192\n"
    "ds_read_b64_tr_b16 a[98:99], v3 offset:12288\n"
    "v_mfma_f32_16x16x32_bf16 v[232:235], a[104:107], v[20:23], v[232:235]\n"
    "v_exp_f32_e32 v25, v25\n"
    "v_mfma_f32_16x16x32_bf16 v[236:239], a[108:111], v[20:23], v[236:239]\n"
    "ds_read_b64_tr_b16 a[100:101], v4 offset:8192\n"
    "v_cvt_pk_bf16_f32 v16, v24, v25\n"
    "v_mfma_f32_16x16x32_bf16 v[240:243], a[112:115], v[20:23], v[240:243]\n"
    "v_exp_f32_e32 v26, v26\n"
    "v_mfma_f32_16x16x32_bf16 v[244:247], a[116:119], v[20:23], v[244:247]\n"
    "ds_read_b64_tr_b16 a[102:103], v4 offset:12288\n"
    "ds_read_b64_tr_b16 a[104:105], v3 offset:8448\n"
    "v_mfma_f32_16x16x32_bf16 v[248:251], a[120:123], v[20:23], v[248:251]\n"
    "v_exp_f32_e32 v27, v27\n"
    "v_mfma_f32_16x16x32_bf16 v[252:255], a[124:127], v[20:23], v[252:255]\n"
    "ds_read_b64_tr_b16 a[106:107], v3 offset:12544\n"
    "v_cvt_pk_bf16_f32 v17, v26, v27\n"
    "v_mfma_f32_16x16x32_bf16 a[144:147], a[128:131], v[20:23], a[144:147]\n"
    "s_waitcnt lgkmcnt(6)\n"
    "v_exp_f32_e32 v28, v28\n"
    "v_mfma_f32_16x16x32_bf16 v[32:35], a[32:35], v[64:67], v[40:43]\n"
    "ds_read_b64_tr_b16 a[108:109], v4 offset:8448\n"
    "ds_read_b64_tr_b16 a[110:111], v4 offset:12544\n"
    "v_mfma_f32_16x16x32_bf16 v[32:35], a[36:39], v[68:71], v[32:35]\n"
    "v_exp_f32_e32 v29, v29\n"
    "v_mfma_f32_16x16x32_bf16 v[32:35], a[40:43], v[72:75], v[32:35]\n"
    "ds_read_b64_tr_b16 a[112:113], v3 offset:8704\n"
    "v_cvt_pk_bf16_f32 v18, v28, v29\n"
    "v_mfma_f32_16x16x32_bf16 v[32:35], a[44:47], v[76:79], v[32:35]\n"
    "v_exp_f32_e32 v30, v30\n"
    "v_mfma_f32_16x16x32_bf16 v[36:39], a[48:51], v[64:67], v[40:43]\n"
    "ds_read_b64_tr_b16 a[114:115], v3 offset:12800\n"
    "ds_read_b64_tr_b16 a[116:117], v4 offset:8704\n"
    "v_mfma_f32_16x16x32_bf16 v[36:39], a[52:55], v[68:71], v[36:39]\n"
    "v_exp_f32_e32 v31, v31\n"
    "v_mfma_f32_16x16x32_bf16 v[36:39], a[56:59], v[72:75], v[36:39]\n"
    "ds_read_b64_tr_b16 a[118:119], v4 offset:12800\n"
    "v_cvt_pk_bf16_f32 v19, v30, v31\n"
    "v_mfma_f32_16x16x32_bf16 v[36:39], a[60:63], v[76:79], v[36:39]\n"
    "ds_read_b64_tr_b16 a[120:121], v3 offset:8960\n"
    "ds_read_b64_tr_b16 a[122:123], v3 offset:13056\n"
    "v_mfma_f32_16x16x32_bf16 v[128:131], a[64:67], v[16:19], v[128:131]\n"
    "v_exp_f32_e32 v32, v32\n"
    "v_mfma_f32_16x16x32_bf16 v[132:135], a[68:71], v[16:19], v[132:135]\n"
    "ds_read_b64_tr_b16 a[124:125], v4 offset:8960\n"
    "ds_read_b64_tr_b16 a[126:127], v4 offset:13056\n"
    "v_mfma_f32_16x16x32_bf16 v[136:139], a[72:75], v[16:19], v[136:139]\n"
    "v_exp_f32_e32 v33, v33\n"
    "v_mfma_f32_16x16x32_bf16 v[140:143], a[76:79], v[16:19], v[140:143]\n"
    "v_cvt_pk_bf16_f32 v20, v32, v33\n"
    "v_and_b32 v60, 15, %[tid]\n"
    "v_mfma_f32_16x16x32_bf16 v[144:147], a[80:83], v[16:19], v[144:147]\n"
    "v_exp_f32_e32 v34, v34\n"
    "v_mfma_f32_16x16x32_bf16 v[148:151], a[84:87], v[16:19], v[148:151]\n"
    "v_bfe_u32 v61, %[tid], 4, 1\n"
    "v_bfe_u32 v62, %[tid], 5, 1\n"
    "v_mfma_f32_16x16x32_bf16 v[152:155], a[88:91], v[16:19], v[152:155]\n"
    "v_exp_f32_e32 v35, v35\n"
    "v_mfma_f32_16x16x32_bf16 v[156:159], a[92:95], v[16:19], v[156:159]\n"
    "v_cvt_pk_bf16_f32 v21, v34, v35\n"
    "s_mov_b32 s43, 0x5d800000\n"
    "v_mfma_f32_16x16x32_bf16 a[132:135], a[128:131], v[16:19], a[132:135]\n"
    "v_exp_f32_e32 v36, v36\n"
    "v_mfma_f32_16x16x32_bf16 v[24:27], a[0:3], v[80:83], v[44:47]\n"
    "s_lshl_b32 s41, s40, 14\n"
    "s_add_u32 s41, s41, 0x18000\n"
    "v_mfma_f32_16x16x32_bf16 v[24:27], a[4:7], v[84:87], v[24:27]\n"
    "v_exp_f32_e32 v37, v37\n"
    "v_mfma_f32_16x16x32_bf16 v[24:27], a[8:11], v[88:91], v[24:27]\n"
    "v_cvt_pk_bf16_f32 v22, v36, v37\n"
    "s_cmp_eq_u32 s40, 0\n"
    "v_mfma_f32_16x16x32_bf16 v[24:27], a[12:15], v[92:95], v[24:27]\n"
    "v_exp_f32_e32 v38, v38\n"
    "v_mfma_f32_16x16x32_bf16 v[28:31], a[16:19], v[80:83], v[44:47]\n"
    "s_cselect_b32 s41, 0xc000, s41\n"
    "v_and_b32 v63, 7, %[tid]\n"
    "v_mfma_f32_16x16x32_bf16 v[28:31], a[20:23], v[84:87], v[28:31]\n"
    "v_exp_f32_e32 v39, v39\n"
    "v_mfma_f32_16x16x32_bf16 v[28:31], a[24:27], v[88:91], v[28:31]\n"
    "v_cvt_pk_bf16_f32 v23, v38, v39\n"
    "v_xor_b32 v63, v63, v62\n"
    "v_mfma_f32_16x16x32_bf16 v[28:31], a[28:31], v[92:95], v[28:31]\n"
    "s_waitcnt lgkmcnt(0)\n"
    "v_lshlrev_b32 v5, 8, v60\n"
    "v_mfma_f32_16x16x32_bf16 v[128:131], a[96:99], v[20:23], v[128:131]\n"
    "v_exp_f32_e32 v24, v24\n"
    "v_mfma_f32_16x16x32_bf16 v[132:135], a[100:103], v[20:23], v[132:135]\n"
    "v_lshl_add_u32 v5, v63, 4, v5\n"
    "v_lshl_add_u32 v5, v61, 3, v5\n"
    "v_mfma_f32_16x16x32_bf16 v[136:139], a[104:107], v[20:23], v[136:139]\n"
    "v_exp_f32_e32 v25, v25\n"
    "v_mfma_f32_16x16x32_bf16 v[140:143], a[108:111], v[20:23], v[140:143]\n"
    "v_cvt_pk_bf16_f32 v16, v24, v25\n"
    "v_add_u32 v5, s41, v5\n"
    "v_mfma_f32_16x16x32_bf16 v[144:147], a[112:115], v[20:23], v[144:147]\n"
    "v_exp_f32_e32 v26, v26\n"
    "v_mfma_f32_16x16x32_bf16 v[148:151], a[116:119], v[20:23], v[148:151]\n"
    "v_xor_b32 v6, 32, v5\n"
    "v_xor_b32 v7, 64, v5\n"
    "v_mfma_f32_16x16x32_bf16 v[152:155], a[120:123], v[20:23], v[152:155]\n"
    "v_exp_f32_e32 v27, v27\n"
    "v_mfma_f32_16x16x32_bf16 v[156:159], a[124:127], v[20:23], v[156:159]\n"
    "v_cvt_pk_bf16_f32 v17, v26, v27\n"
    "v_xor_b32 v8, 0x60, v5\n"
    "v_mfma_f32_16x16x32_bf16 a[132:135], a[128:131], v[20:23], a[132:135]\n"
    "v_exp_f32_e32 v28, v28\n"
    "v_mfma_f32_16x16x32_bf16 v[32:35], a[32:35], v[80:83], v[44:47]\n"
    "v_and_b32 v60, 63, %[tid]\n"
    "v_lshlrev_b32 v9, 4, v60\n"
    "v_and_b32 v60, 48, v60\n"
    "v_xor_b32 v2, v60, v9\n"
    "v_mfma_f32_16x16x32_bf16 v[32:35], a[36:39], v[84:87], v[32:35]\n"
    "v_exp_f32_e32 v29, v29\n"
    "v_add_u32 v2, s41, v2\n"
    "v_xor_b32 v63, 64, v2\n"
    "v_mfma_f32_16x16x32_bf16 v[32:35], a[40:43], v[88:91], v[32:35]\n"
    "v_cvt_pk_bf16_f32 v18, v28, v29\n"
    "v_mfma_f32_16x16x32_bf16 v[32:35], a[44:47], v[92:95], v[32:35]\n"
    "v_exp_f32_e32 v30, v30\n"
    "v_mfma_f32_16x16x32_bf16 v[36:39], a[48:51], v[80:83], v[44:47]\n"
    "v_mfma_f32_16x16x32_bf16 v[36:39], a[52:55], v[84:87], v[36:39]\n"
    "v_exp_f32_e32 v31, v31\n"
    "v_accvgpr_read_b32 v60, a132\n"
    "v_rcp_f32 v10, v60\n"
    "v_mfma_f32_16x16x32_bf16 v[36:39], a[56:59], v[88:91], v[36:39]\n"
    "v_cvt_pk_bf16_f32 v19, v30, v31\n"
    "v_cmp_nlt_f32_e64 s[52:53], v60, s43\n"
    "v_mul_f32 v128, v10, v128\n"
    "v_mul_f32 v129, v10, v129\n"
    "v_mfma_f32_16x16x32_bf16 v[36:39], a[60:63], v[92:95], v[36:39]\n"
    "v_mul_f32 v130, v10, v130\n"
    "v_mul_f32 v131, v10, v131\n"
    "v_mul_f32 v132, v10, v132\n"
    "v_mul_f32 v133, v10, v133\n"
    "v_mfma_f32_16x16x32_bf16 v[160:163], a[64:67], v[16:19], v[160:163]\n"
    "v_exp_f32_e32 v32, v32\n"
    "v_mul_f32 v134, v10, v134\n"
    "v_mul_f32 v135, v10, v135\n"
    "v_mfma_f32_16x16x32_bf16 v[164:167], a[68:71], v[16:19], v[164:167]\n"
    "v_cvt_pk_bf16_f32 v128, v128, v129\n"
    "v_cvt_pk_bf16_f32 v129, v130, v131\n"
    "v_cvt_pk_bf16_f32 v130, v132, v133\n"
    "v_cvt_pk_bf16_f32 v131, v134, v135\n"
    "v_mfma_f32_16x16x32_bf16 v[168:171], a[72:75], v[16:19], v[168:171]\n"
    "v_exp_f32_e32 v33, v33\n"
    "v_mul_f32 v136, v10, v136\n"
    "v_mul_f32 v137, v10, v137\n"
    "v_mfma_f32_16x16x32_bf16 v[172:175], a[76:79], v[16:19], v[172:175]\n"
    "v_cvt_pk_bf16_f32 v20, v32, v33\n"
    "v_mul_f32 v138, v10, v138\n"
    "v_mul_f32 v139, v10, v139\n"
    "ds_write_b64 v5, v[128:129]\n"
    "v_mfma_f32_16x16x32_bf16 v[176:179], a[80:83], v[16:19], v[176:179]\n"
    "v_exp_f32_e32 v34, v34\n"
    "ds_write_b64 v6, v[130:131]\n"
    "v_mul_f32 v140, v10, v140\n"
    "v_mfma_f32_16x16x32_bf16 v[180:183], a[84:87], v[16:19], v[180:183]\n"
    "v_mul_f32 v141, v10, v141\n"
    "v_mul_f32 v142, v10, v142\n"
    "v_mul_f32 v143, v10, v143\n"
    "v_cvt_pk_bf16_f32 v136, v136, v137\n"
    "v_mfma_f32_16x16x32_bf16 v[184:187], a[88:91], v[16:19], v[184:187]\n"
    "v_exp_f32_e32 v35, v35\n"
    "v_cvt_pk_bf16_f32 v137, v138, v139\n"
    "v_cvt_pk_bf16_f32 v138, v140, v141\n"
    "v_mfma_f32_16x16x32_bf16 v[188:191], a[92:95], v[16:19], v[188:191]\n"
    "v_cvt_pk_bf16_f32 v21, v34, v35\n"
    "v_cvt_pk_bf16_f32 v139, v142, v143\n"
    "v_mul_f32 v144, v10, v144\n"
    "v_mul_f32 v145, v10, v145\n"
    "v_mfma_f32_16x16x32_bf16 a[136:139], a[128:131], v[16:19], a[136:139]\n"
    "v_exp_f32_e32 v36, v36\n"
    "v_mul_f32 v146, v10, v146\n"
    "v_mul_f32 v147, v10, v147\n"
    "v_mfma_f32_16x16x32_bf16 v[24:27], a[0:3], v[96:99], v[48:51]\n"
    "ds_write_b64 v7, v[136:137]\n"
    "ds_write_b64 v8, v[138:139]\n"
    "v_mul_f32 v148, v10, v148\n"
    "v_mul_f32 v149, v10, v149\n"
    "v_mfma_f32_16x16x32_bf16 v[24:27], a[4:7], v[100:103], v[24:27]\n"
    "v_exp_f32_e32 v37, v37\n"
    "v_mul_f32 v150, v10, v150\n"
    "v_mul_f32 v151, v10, v151\n"
    "v_mfma_f32_16x16x32_bf16 v[24:27], a[8:11], v[104:107], v[24:27]\n"
    "v_cvt_pk_bf16_f32 v22, v36, v37\n"
    "v_cvt_pk_bf16_f32 v144, v144, v145\n"
    "v_cvt_pk_bf16_f32 v145, v146, v147\n"
    "v_cvt_pk_bf16_f32 v146, v148, v149\n"
    "v_mfma_f32_16x16x32_bf16 v[24:27], a[12:15], v[108:111], v[24:27]\n"
    "v_exp_f32_e32 v38, v38\n"
    "v_cvt_pk_bf16_f32 v147, v150, v151\n"
    "v_mul_f32 v152, v10, v152\n"
    "v_mfma_f32_16x16x32_bf16 v[28:31], a[16:19], v[96:99], v[48:51]\n"
    "v_mul_f32 v153, v10, v153\n"
    "v_mul_f32 v154, v10, v154\n"
    "v_mul_f32 v155, v10, v155\n"
    "ds_write_b64 v5, v[144:145] offset:128\n"
    "v_mfma_f32_16x16x32_bf16 v[28:31], a[20:23], v[100:103], v[28:31]\n"
    "v_exp_f32_e32 v39, v39\n"
    "ds_write_b64 v6, v[146:147] offset:128\n"
    "v_mul_f32 v156, v10, v156\n"
    "v_mfma_f32_16x16x32_bf16 v[28:31], a[24:27], v[104:107], v[28:31]\n"
    "v_cvt_pk_bf16_f32 v23, v38, v39\n"
    "v_mul_f32 v157, v10, v157\n"
    "v_mul_f32 v158, v10, v158\n"
    "v_mul_f32 v159, v10, v159\n"
    "v_mfma_f32_16x16x32_bf16 v[28:31], a[28:31], v[108:111], v[28:31]\n"
    "v_cvt_pk_bf16_f32 v152, v152, v153\n"
    "v_cvt_pk_bf16_f32 v153, v154, v155\n"
    "v_cvt_pk_bf16_f32 v154, v156, v157\n"
    "v_cvt_pk_bf16_f32 v155, v158, v159\n"
    "v_mfma_f32_16x16x32_bf16 v[160:163], a[96:99], v[20:23], v[160:163]\n"
    "v_exp_f32_e32 v24, v24\n"
    "ds_write_b64 v7, v[152:153] offset:128\n"
    "ds_write_b64 v8, v[154:155] offset:128\n"
    "v_mfma_f32_16x16x32_bf16 v[164:167], a[100:103], v[20:23], v[164:167]\n"
    "ds_read_b128 v[64:67], v2\n"
    "ds_read_b128 v[68:71], v63 offset:1024\n"
    "ds_read_b128 v[72:75], v2 offset:2048\n"
    "ds_read_b128 v[76:79], v63 offset:3072\n"
    "v_mfma_f32_16x16x32_bf16 v[168:171], a[104:107], v[20:23], v[168:171]\n"
    "v_exp_f32_e32 v25, v25\n"
    "v_mfma_f32_16x16x32_bf16 v[172:175], a[108:111], v[20:23], v[172:175]\n"
    "v_cvt_pk_bf16_f32 v16, v24, v25\n"
    "v_mfma_f32_16x16x32_bf16 v[176:179], a[112:115], v[20:23], v[176:179]\n"
    "v_exp_f32_e32 v26, v26\n"
    "v_mfma_f32_16x16x32_bf16 v[180:183], a[116:119], v[20:23], v[180:183]\n"
    "v_mfma_f32_16x16x32_bf16 v[184:187], a[120:123], v[20:23], v[184:187]\n"
    "v_exp_f32_e32 v27, v27\n"
    "v_mfma_f32_16x16x32_bf16 v[188:191], a[124:127], v[20:23], v[188:191]\n"
    "v_cvt_pk_bf16_f32 v17, v26, v27\n"
    "v_mfma_f32_16x16x32_bf16 a[136:139], a[128:131], v[20:23], a[136:139]\n"
    "v_exp_f32_e32 v28, v28\n"
    "v_mfma_f32_16x16x32_bf16 v[32:35], a[32:35], v[96:99], v[48:51]\n"
    "s_waitcnt lgkmcnt(0)\n"
    "buffer_store_dwordx4 v[64:67], v9, s[44:47], 0 offen sc0 sc1 nt\n"
    "v_mfma_f32_16x16x32_bf16 v[32:35], a[36:39], v[100:103], v[32:35]\n"
    "v_exp_f32_e32 v29, v29\n"
    "buffer_store_dwordx4 v[68:71], v9, s[44:47], 0 offen offset:1024 sc0 sc1 nt\n"
    "v_mfma_f32_16x16x32_bf16 v[32:35], a[40:43], v[104:107], v[32:35]\n"
    "v_cvt_pk_bf16_f32 v18, v28, v29\n"
    "buffer_store_dwordx4 v[72:75], v9, s[44:47], 0 offen offset:2048 sc0 sc1 nt\n"
    "v_mfma_f32_16x16x32_bf16 v[32:35], a[44:47], v[108:111], v[32:35]\n"
    "v_exp_f32_e32 v30, v30\n"
    "buffer_store_dwordx4 v[76:79], v9, s[44:47], 0 offen offset:3072 sc0 sc1 nt\n"
    "v_mfma_f32_16x16x32_bf16 v[36:39], a[48:51], v[96:99], v[48:51]\n"
    "v_mfma_f32_16x16x32_bf16 v[36:39], a[52:55], v[100:103], v[36:39]\n"
    "v_exp_f32_e32 v31, v31\n"
    "v_accvgpr_read_b32 v60, a136\n"
    "v_rcp_f32 v12, v60\n"
    "v_mfma_f32_16x16x32_bf16 v[36:39], a[56:59], v[104:107], v[36:39]\n"
    "v_cvt_pk_bf16_f32 v19, v30, v31\n"
    "v_cmp_nlt_f32_e64 s[54:55], v60, s43\n"
    "v_mul_f32 v160, v12, v160\n"
    "v_mul_f32 v161, v12, v161\n"
    "v_mfma_f32_16x16x32_bf16 v[36:39], a[60:63], v[108:111], v[36:39]\n"
    "v_mul_f32 v162, v12, v162\n"
    "v_mul_f32 v163, v12, v163\n"
    "v_mul_f32 v164, v12, v164\n"
    "v_mul_f32 v165, v12, v165\n"
    "v_mfma_f32_16x16x32_bf16 v[192:195], a[64:67], v[16:19], v[192:195]\n"
    "v_exp_f32_e32 v32, v32\n"
    "v_mul_f32 v166, v12, v166\n"
    "v_mul_f32 v167, v12, v167\n"
    "v_mfma_f32_16x16x32_bf16 v[196:199], a[68:71], v[16:19], v[196:199]\n"
    "v_cvt_pk_bf16_f32 v160, v160, v161\n"
    "v_cvt_pk_bf16_f32 v161, v162, v163\n"
    "v_cvt_pk_bf16_f32 v162, v164, v165\n"
    "v_cvt_pk_bf16_f32 v163, v166, v167\n"
    "v_mfma_f32_16x16x32_bf16 v[200:203], a[72:75], v[16:19], v[200:203]\n"
    "v_exp_f32_e32 v33, v33\n"
    "v_mul_f32 v168, v12, v168\n"
    "v_mul_f32 v169, v12, v169\n"
    "v_mfma_f32_16x16x32_bf16 v[204:207], a[76:79], v[16:19], v[204:207]\n"
    "v_cvt_pk_bf16_f32 v20, v32, v33\n"
    "v_mul_f32 v170, v12, v170\n"
    "v_mul_f32 v171, v12, v171\n"
    "ds_write_b64 v5, v[160:161] offset:4096\n"
    "v_mfma_f32_16x16x32_bf16 v[208:211], a[80:83], v[16:19], v[208:211]\n"
    "v_exp_f32_e32 v34, v34\n"
    "ds_write_b64 v6, v[162:163] offset:4096\n"
    "v_mul_f32 v172, v12, v172\n"
    "v_mfma_f32_16x16x32_bf16 v[212:215], a[84:87], v[16:19], v[212:215]\n"
    "v_mul_f32 v173, v12, v173\n"
    "v_mul_f32 v174, v12, v174\n"
    "v_mul_f32 v175, v12, v175\n"
    "v_cvt_pk_bf16_f32 v168, v168, v169\n"
    "v_mfma_f32_16x16x32_bf16 v[216:219], a[88:91], v[16:19], v[216:219]\n"
    "v_exp_f32_e32 v35, v35\n"
    "v_cvt_pk_bf16_f32 v169, v170, v171\n"
    "v_cvt_pk_bf16_f32 v170, v172, v173\n"
    "v_mfma_f32_16x16x32_bf16 v[220:223], a[92:95], v[16:19], v[220:223]\n"
    "v_cvt_pk_bf16_f32 v21, v34, v35\n"
    "v_cvt_pk_bf16_f32 v171, v174, v175\n"
    "v_mul_f32 v176, v12, v176\n"
    "v_mul_f32 v177, v12, v177\n"
    "v_mfma_f32_16x16x32_bf16 a[140:143], a[128:131], v[16:19], a[140:143]\n"
    "v_exp_f32_e32 v36, v36\n"
    "v_mul_f32 v178, v12, v178\n"
    "v_mul_f32 v179, v12, v179\n"
    "v_mfma_f32_16x16x32_bf16 v[24:27], a[0:3], v[112:115], v[52:55]\n"
    "ds_write_b64 v7, v[168:169] offset:4096\n"
    "ds_write_b64 v8, v[170:171] offset:4096\n"
    "v_mul_f32 v180, v12, v180\n"
    "v_mul_f32 v181, v12, v181\n"
    "v_mfma_f32_16x16x32_bf16 v[24:27], a[4:7], v[116:119], v[24:27]\n"
    "v_exp_f32_e32 v37, v37\n"
    "v_mul_f32 v182, v12, v182\n"
    "v_mul_f32 v183, v12, v183\n"
    "v_mfma_f32_16x16x32_bf16 v[24:27], a[8:11], v[120:123], v[24:27]\n"
    "v_cvt_pk_bf16_f32 v22, v36, v37\n"
    "v_cvt_pk_bf16_f32 v176, v176, v177\n"
    "v_cvt_pk_bf16_f32 v177, v178, v179\n"
    "v_cvt_pk_bf16_f32 v178, v180, v181\n"
    "v_mfma_f32_16x16x32_bf16 v[24:27], a[12:15], v[124:127], v[24:27]\n"
    "v_exp_f32_e32 v38, v38\n"
    "v_cvt_pk_bf16_f32 v179, v182, v183\n"
    "v_mul_f32 v184, v12, v184\n"
    "v_mfma_f32_16x16x32_bf16 v[28:31], a[16:19], v[112:115], v[52:55]\n"
    "v_mul_f32 v185, v12, v185\n"
    "v_mul_f32 v186, v12, v186\n"
    "v_mul_f32 v187, v12, v187\n"
    "ds_write_b64 v5, v[176:177] offset:4224\n"
    "v_mfma_f32_16x16x32_bf16 v[28:31], a[20:23], v[116:119], v[28:31]\n"
    "v_exp_f32_e32 v39, v39\n"
    "ds_write_b64 v6, v[178:179] offset:4224\n"
    "v_mul_f32 v188, v12, v188\n"
    "v_mfma_f32_16x16x32_bf16 v[28:31], a[24:27], v[120:123], v[28:31]\n"
    "v_cvt_pk_bf16_f32 v23, v38, v39\n"
    "v_mul_f32 v189, v12, v189\n"
    "v_mul_f32 v190, v12, v190\n"
    "v_mul_f32 v191, v12, v191\n"
    "v_mfma_f32_16x16x32_bf16 v[28:31], a[28:31], v[124:127], v[28:31]\n"
    "v_cvt_pk_bf16_f32 v184, v184, v185\n"
    "v_cvt_pk_bf16_f32 v185, v186, v187\n"
    "v_cvt_pk_bf16_f32 v186, v188, v189\n"
    "v_cvt_pk_bf16_f32 v187, v190, v191\n"
    "v_mfma_f32_16x16x32_bf16 v[192:195], a[96:99], v[20:23], v[192:195]\n"
    "v_exp_f32_e32 v24, v24\n"
    "ds_write_b64 v7, v[184:185] offset:4224\n"
    "ds_write_b64 v8, v[186:187] offset:4224\n"
    "v_mfma_f32_16x16x32_bf16 v[196:199], a[100:103], v[20:23], v[196:199]\n"
    "ds_read_b128 v[80:83], v2 offset:4096\n"
    "ds_read_b128 v[84:87], v63 offset:5120\n"
    "ds_read_b128 v[88:91], v2 offset:6144\n"
    "ds_read_b128 v[92:95], v63 offset:7168\n"
    "v_mfma_f32_16x16x32_bf16 v[200:203], a[104:107], v[20:23], v[200:203]\n"
    "v_exp_f32_e32 v25, v25\n"
    "v_mfma_f32_16x16x32_bf16 v[204:207], a[108:111], v[20:23], v[204:207]\n"
    "v_cvt_pk_bf16_f32 v16, v24, v25\n"
    "v_mfma_f32_16x16x32_bf16 v[208:211], a[112:115], v[20:23], v[208:211]\n"
    "v_exp_f32_e32 v26, v26\n"
    "v_mfma_f32_16x16x32_bf16 v[212:215], a[116:119], v[20:23], v[212:215]\n"
    "v_mfma_f32_16x16x32_bf16 v[216:219], a[120:123], v[20:23], v[216:219]\n"
    "v_exp_f32_e32 v27, v27\n"
    "v_mfma_f32_16x16x32_bf16 v[220:223], a[124:127], v[20:23], v[220:223]\n"
    "v_cvt_pk_bf16_f32 v17, v26, v27\n"
    "v_mfma_f32_16x16x32_bf16 a[140:143], a[128:131], v[20:23], a[140:143]\n"
    "v_exp_f32_e32 v28, v28\n"
    "v_mfma_f32_16x16x32_bf16 v[32:35], a[32:35], v[112:115], v[52:55]\n"
    "s_waitcnt lgkmcnt(0)\n"
    "buffer_store_dwordx4 v[80:83], v9, s[44:47], s48 offen sc0 sc1 nt\n"
    "v_mfma_f32_16x16x32_bf16 v[32:35], a[36:39], v[116:119], v[32:35]\n"
    "v_exp_f32_e32 v29, v29\n"
    "buffer_store_dwordx4 v[84:87], v9, s[44:47], s48 offen offset:1024 sc0 sc1 nt\n"
    "v_mfma_f32_16x16x32_bf16 v[32:35], a[40:43], v[120:123], v[32:35]\n"
    "v_cvt_pk_bf16_f32 v18, v28, v29\n"
    "buffer_store_dwordx4 v[88:91], v9, s[44:47], s48 offen offset:2048 sc0 sc1 nt\n"
    "v_mfma_f32_16x16x32_bf16 v[32:35], a[44:47], v[124:127], v[32:35]\n"
    "v_exp_f32_e32 v30, v30\n"
    "buffer_store_dwordx4 v[92:95], v9, s[44:47], s48 offen offset:3072 sc0 sc1 nt\n"
    "v_mfma_f32_16x16x32_bf16 v[36:39], a[48:51], v[112:115], v[52:55]\n"
    "v_mfma_f32_16x16x32_bf16 v[36:39], a[52:55], v[116:119], v[36:39]\n"
    "v_exp_f32_e32 v31, v31\n"
    "v_accvgpr_read_b32 v60, a140\n"
    "v_rcp_f32 v14, v60\n"
    "v_mfma_f32_16x16x32_bf16 v[36:39], a[56:59], v[120:123], v[36:39]\n"
    "v_cvt_pk_bf16_f32 v19, v30, v31\n"
    "v_cmp_nlt_f32_e64 s[56:57], v60, s43\n"
    "v_mul_f32 v192, v14, v192\n"
    "v_mul_f32 v193, v14, v193\n"
    "v_mfma_f32_16x16x32_bf16 v[36:39], a[60:63], v[124:127], v[36:39]\n"
    "v_mul_f32 v194, v14, v194\n"
    "v_mul_f32 v195, v14, v195\n"
    "v_mul_f32 v196, v14, v196\n"
    "v_mul_f32 v197, v14, v197\n"
    "v_mfma_f32_16x16x32_bf16 v[224:227], a[64:67], v[16:19], v[224:227]\n"
    "v_exp_f32_e32 v32, v32\n"
    "v_mul_f32 v198, v14, v198\n"
    "v_mul_f32 v199, v14, v199\n"
    "v_mfma_f32_16x16x32_bf16 v[228:231], a[68:71], v[16:19], v[228:231]\n"
    "v_cvt_pk_bf16_f32 v192, v192, v193\n"
    "v_cvt_pk_bf16_f32 v193, v194, v195\n"
    "v_cvt_pk_bf16_f32 v194, v196, v197\n"
    "v_cvt_pk_bf16_f32 v195, v198, v199\n"
    "v_mfma_f32_16x16x32_bf16 v[232:235], a[72:75], v[16:19], v[232:235]\n"
    "v_exp_f32_e32 v33, v33\n"
    "v_mul_f32 v200, v14, v200\n"
    "v_mul_f32 v201, v14, v201\n"
    "v_mfma_f32_16x16x32_bf16 v[236:239], a[76:79], v[16:19], v[236:239]\n"
    "v_cvt_pk_bf16_f32 v20, v32, v33\n"
    "v_mul_f32 v202, v14, v202\n"
    "v_mul_f32 v203, v14, v203\n"
    "ds_write_b64 v5, v[192:193] offset:8192\n"
    "v_mfma_f32_16x16x32_bf16 v[240:243], a[80:83], v[16:19], v[240:243]\n"
    "v_exp_f32_e32 v34, v34\n"
    "ds_write_b64 v6, v[194:195] offset:8192\n"
    "v_mul_f32 v204, v14, v204\n"
    "v_mfma_f32_16x16x32_bf16 v[244:247], a[84:87], v[16:19], v[244:247]\n"
    "v_mul_f32 v205, v14, v205\n"
    "v_mul_f32 v206, v14, v206\n"
    "v_mul_f32 v207, v14, v207\n"
    "v_cvt_pk_bf16_f32 v200, v200, v201\n"
    "v_mfma_f32_16x16x32_bf16 v[248:251], a[88:91], v[16:19], v[248:251]\n"
    "v_exp_f32_e32 v35, v35\n"
    "v_cvt_pk_bf16_f32 v201, v202, v203\n"
    "v_cvt_pk_bf16_f32 v202, v204, v205\n"
    "v_mfma_f32_16x16x32_bf16 v[252:255], a[92:95], v[16:19], v[252:255]\n"
    "v_cvt_pk_bf16_f32 v21, v34, v35\n"
    "v_cvt_pk_bf16_f32 v203, v206, v207\n"
    "v_mul_f32 v208, v14, v208\n"
    "v_mul_f32 v209, v14, v209\n"
    "v_mfma_f32_16x16x32_bf16 a[144:147], a[128:131], v[16:19], a[144:147]\n"
    "v_exp_f32_e32 v36, v36\n"
    "v_mul_f32 v210, v14, v210\n"
    "v_mul_f32 v211, v14, v211\n"
    "v_exp_f32_e32 v37, v37\n"
    "ds_write_b64 v7, v[200:201] offset:8192\n"
    "v_cvt_pk_bf16_f32 v22, v36, v37\n"
    "v_exp_f32_e32 v38, v38\n"
    "ds_write_b64 v8, v[202:203] offset:8192\n"
    "v_exp_f32_e32 v39, v39\n"
    "v_mul_f32 v212, v14, v212\n"
    "v_cvt_pk_bf16_f32 v23, v38, v39\n"
    "v_mul_f32 v213, v14, v213\n"
    "v_mul_f32 v214, v14, v214\n"
    "v_mfma_f32_16x16x32_bf16 v[224:227], a[96:99], v[20:23], v[224:227]\n"
    "v_mul_f32 v215, v14, v215\n"
    "v_cvt_pk_bf16_f32 v208, v208, v209\n"
    "v_cvt_pk_bf16_f32 v209, v210, v211\n"
    "v_cvt_pk_bf16_f32 v210, v212, v213\n"
    "v_mfma_f32_16x16x32_bf16 v[228:231], a[100:103], v[20:23], v[228:231]\n"
    "v_cvt_pk_bf16_f32 v211, v214, v215\n"
    "v_mul_f32 v216, v14, v216\n"
    "v_mul_f32 v217, v14, v217\n"
    "v_mul_f32 v218, v14, v218\n"
    "v_mfma_f32_16x16x32_bf16 v[232:235], a[104:107], v[20:23], v[232:235]\n"
    "v_mul_f32 v219, v14, v219\n"
    "ds_write_b64 v5, v[208:209] offset:8320\n"
    "ds_write_b64 v6, v[210:211] offset:8320\n"
    "v_mul_f32 v220, v14, v220\n"
    "v_mfma_f32_16x16x32_bf16 v[236:239], a[108:111], v[20:23], v[236:239]\n"
    "v_mul_f32 v221, v14, v221\n"
    "v_mul_f32 v222, v14, v222\n"
    "v_mul_f32 v223, v14, v223\n"
    "v_cvt_pk_bf16_f32 v216, v216, v217\n"
    "v_mfma_f32_16x16x32_bf16 v[240:243], a[112:115], v[20:23], v[240:243]\n"
    "v_cvt_pk_bf16_f32 v217, v218, v219\n"
    "v_cvt_pk_bf16_f32 v218, v220, v221\n"
    "v_cvt_pk_bf16_f32 v219, v222, v223\n"
    "ds_write_b64 v7, v[216:217] offset:8320\n"
    "v_mfma_f32_16x16x32_bf16 v[244:247], a[116:119], v[20:23], v[244:247]\n"
    "ds_write_b64 v8, v[218:219] offset:8320\n"
    "ds_read_b128 v[96:99], v2 offset:8192\n"
    "ds_read_b128 v[100:103], v63 offset:9216\n"
    "ds_read_b128 v[104:107], v2 offset:10240\n"
    "v_mfma_f32_16x16x32_bf16 v[248:251], a[120:123], v[20:23], v[248:251]\n"
    "ds_read_b128 v[108:111], v63 offset:11264\n"
    "v_mfma_f32_16x16x32_bf16 v[252:255], a[124:127], v[20:23], v[252:255]\n"
    "v_mfma_f32_16x16x32_bf16 a[144:147], a[128:131], v[20:23], a[144:147]\n"
    "s_waitcnt lgkmcnt(0)\n"
    "buffer_store_dwordx4 v[96:99], v9, s[44:47], s49 offen sc0 sc1 nt\n"
    "buffer_store_dwordx4 v[100:103], v9, s[44:47], s49 offen offset:1024 sc0 sc1 nt\n"
    "buffer_store_dwordx4 v[104:107], v9, s[44:47], s49 offen offset:2048 sc0 sc1 nt\n"
    "buffer_store_dwordx4 v[108:111], v9, s[44:47], s49 offen offset:3072 sc0 sc1 nt\n"
    "s_nop 2\n"
    "v_accvgpr_read_b32 v60, a144\n"
    "v_rcp_f32 v58, v60\n"
    "v_cmp_nlt_f32_e64 s[58:59], v60, s43\n"
    "v_pk_mul_f32 v[224:225], v[224:225], v[58:59] op_sel_hi:[1,0]\n"
    "v_pk_mul_f32 v[226:227], v[226:227], v[58:59] op_sel_hi:[1,0]\n"
    "v_pk_mul_f32 v[228:229], v[228:229], v[58:59] op_sel_hi:[1,0]\n"
    "v_pk_mul_f32 v[230:231], v[230:231], v[58:59] op_sel_hi:[1,0]\n"
    "v_cvt_pk_bf16_f32 v224, v224, v225\n"
    "v_cvt_pk_bf16_f32 v225, v226, v227\n"
    "v_cvt_pk_bf16_f32 v226, v228, v229\n"
    "v_cvt_pk_bf16_f32 v227, v230, v231\n"
    "v_pk_mul_f32 v[232:233], v[232:233], v[58:59] op_sel_hi:[1,0]\n"
    "v_pk_mul_f32 v[234:235], v[234:235], v[58:59] op_sel_hi:[1,0]\n"
    "ds_write_b64 v5, v[224:225] offset:12288\n"
    "ds_write_b64 v6, v[226:227] offset:12288\n"
    "v_pk_mul_f32 v[236:237], v[236:237], v[58:59] op_sel_hi:[1,0]\n"
    "v_pk_mul_f32 v[238:239], v[238:239], v[58:59] op_sel_hi:[1,0]\n"
    "v_cvt_pk_bf16_f32 v232, v232, v233\n"
    "v_cvt_pk_bf16_f32 v233, v234, v235\n"
    "v_cvt_pk_bf16_f32 v234, v236, v237\n"
    "v_cvt_pk_bf16_f32 v235, v238, v239\n"
    "v_pk_mul_f32 v[240:241], v[240:241], v[58:59] op_sel_hi:[1,0]\n"
    "v_pk_mul_f32 v[242:243], v[242:243], v[58:59] op_sel_hi:[1,0]\n"
    "ds_write_b64 v7, v[232:233] offset:12288\n"
    "ds_write_b64 v8, v[234:235] offset:12288\n"
    "v_pk_mul_f32 v[244:245], v[244:245], v[58:59] op_sel_hi:[1,0]\n"
    "v_pk_mul_f32 v[246:247], v[246:247], v[58:59] op_sel_hi:[1,0]\n"
    "v_cvt_pk_bf16_f32 v240, v240, v241\n"
    "v_cvt_pk_bf16_f32 v241, v242, v243\n"
    "v_cvt_pk_bf16_f32 v242, v244, v245\n"
    "v_cvt_pk_bf16_f32 v243, v246, v247\n"
    "v_pk_mul_f32 v[248:249], v[248:249], v[58:59] op_sel_hi:[1,0]\n"
    "v_pk_mul_f32 v[250:251], v[250:251], v[58:59] op_sel_hi:[1,0]\n"
    "ds_write_b64 v5, v[240:241] offset:12416\n"
    "ds_write_b64 v6, v[242:243] offset:12416\n"
    "v_pk_mul_f32 v[252:253], v[252:253], v[58:59] op_sel_hi:[1,0]\n"
    "v_pk_mul_f32 v[254:255], v[254:255], v[58:59] op_sel_hi:[1,0]\n"
    "v_cvt_pk_bf16_f32 v248, v248, v249\n"
    "v_cvt_pk_bf16_f32 v249, v250, v251\n"
    "v_cvt_pk_bf16_f32 v250, v252, v253\n"
    "v_cvt_pk_bf16_f32 v251, v254, v255\n"
    "ds_write_b64 v7, v[248:249] offset:12416\n"
    "ds_write_b64 v8, v[250:251] offset:12416\n"
    "ds_read_b128 v[112:115], v2 offset:12288\n"
    "ds_read_b128 v[116:119], v63 offset:13312\n"
    "ds_read_b128 v[120:123], v2 offset:14336\n"
    "ds_read_b128 v[124:127], v63 offset:15360\n"
    "s_waitcnt lgkmcnt(3)\n"
    "buffer_store_dwordx4 v[112:115], v9, s[44:47], s50 offen sc0 sc1 nt\n"
    "s_waitcnt lgkmcnt(2)\n"
    "buffer_store_dwordx4 v[116:119], v9, s[44:47], s50 offen offset:1024 sc0 sc1 nt\n"
    "s_waitcnt lgkmcnt(1)\n"
    "buffer_store_dwordx4 v[120:123], v9, s[44:47], s50 offen offset:2048 sc0 sc1 nt\n"
    "s_waitcnt lgkmcnt(0)\n"
    "buffer_store_dwordx4 v[124:127], v9, s[44:47], s50 offen offset:3072 sc0 sc1 nt\n"
    "s_cmp_lg_u64 s[52:53], 0\n"
    "s_cselect_b32 s52, 1, 0\n"
    "s_cmp_lg_u64 s[54:55], 0\n"
    "s_cselect_b32 s54, 2, 0\n"
    "s_cmp_lg_u64 s[56:57], 0\n"
    "s_cselect_b32 s56, 4, 0\n"
    "s_cmp_lg_u64 s[58:59], 0\n"
    "s_cselect_b32 s58, 8, 0\n"
    "s_or_b32 s52, s52, s54\n"
    "s_or_b32 s56, s56, s58\n"
    "s_or_b32 s52, s52, s56\n"
    "s_mov_b32 %[flag], s52\n"
    ".Lattn_end_%=:\n"
    : [flag] "=&s"(flag)
    : [ka] "s"(__builtin_amdgcn_kernarg_segment_ptr()), [bid] "s"(blockIdx.x), [tid] "v"(threadIdx.x), [ldsb] "s"(ldsb)
    :
      "v2", "v3", "v4", "v5", "v6", "v7", "v8", "v9", "v10", "v11", "v12", "v13", "v14", "v15", "v16", "v17", "v18", "v19", "v20", "v21", "v22", "v23", "v24", "v25",
      "v26", "v27", "v28", "v29", "v30", "v31", "v32", "v33", "v34", "v35", "v36", "v37", "v38", "v39", "v40", "v41", "v42", "v43", "v44", "v45", "v46", "v47", "v48", "v49",
      "v50", "v51", "v52", "v53", "v54", "v55", "v56", "v57", "v58", "v59", "v60", "v61", "v62", "v63", "v64", "v65", "v66", "v67", "v68", "v69", "v70", "v71", "v72", "v73",
      "v74", "v75", "v76", "v77", "v78", "v79", "v80", "v81", "v82", "v83", "v84", "v85", "v86", "v87", "v88", "v89", "v90", "v91", "v92", "v93", "v94", "v95", "v96", "v97",
      "v98", "v99", "v100", "v101", "v102", "v103", "v104", "v105", "v106", "v107", "v108", "v109", "v110", "v111", "v112", "v113", "v114", "v115", "v116", "v117", "v118", "v119", "v120", "v121",
      "v122", "v123", "v124", "v125", "v126", "v127", "v128", "v129", "v130", "v131", "v132", "v133", "v134", "v135", "v136", "v137", "v138", "v139", "v140", "v141", "v142", "v143", "v144", "v145",
      "v146", "v147", "v148", "v149", "v150", "v151", "v152", "v153", "v154", "v155", "v156", "v157", "v158", "v159", "v160", "v161", "v162", "v163", "v164", "v165", "v166", "v167", "v168", "v169",
      "v170", "v171", "v172", "v173", "v174", "v175", "v176", "v177", "v178", "v179", "v180", "v181", "v182", "v183", "v184", "v185", "v186", "v187", "v188", "v189", "v190", "v191", "v192", "v193",
      "v194", "v195", "v196", "v197", "v198", "v199", "v200", "v201", "v202", "v203", "v204", "v205", "v206", "v207", "v208", "v209", "v210", "v211", "v212", "v213", "v214", "v215", "v216", "v217",
      "v218", "v219", "v220", "v221", "v222", "v223", "v224", "v225", "v226", "v227", "v228", "v229", "v230", "v231", "v232", "v233", "v234", "v235", "v236", "v237", "v238", "v239", "v240", "v241",
      "v242", "v243", "v244", "v245", "v246", "v247", "v248", "v249", "v250", "v251", "v252", "v253", "v254", "v255", "a0", "a1", "a2", "a3", "a4", "a5", "a6", "a7", "a8", "a9",
      "a10", "a11", "a12", "a13", "a14", "a15", "a16", "a17", "a18", "a19", "a20", "a21", "a22", "a23", "a24", "a25", "a26", "a27", "a28", "a29", "a30", "a31", "a32", "a33",
      "a34", "a35", "a36", "a37", "a38", "a39", "a40", "a41", "a42", "a43", "a44", "a45", "a46", "a47", "a48", "a49", "a50", "a51", "a52", "a53", "a54", "a55", "a56", "a57",
      "a58", "a59", "a60", "a61", "a62", "a63", "a64", "a65", "a66", "a67", "a68", "a69", "a70", "a71", "a72", "a73", "a74", "a75", "a76", "a77", "a78", "a79", "a80", "a81",
      "a82", "a83", "a84", "a85", "a86", "a87", "a88", "a89", "a90", "a91", "a92", "a93", "a94", "a95", "a96", "a97", "a98", "a99", "a100", "a101", "a102", "a103", "a104", "a105",
      "a106", "a107", "a108", "a109", "a110", "a111", "a112", "a113", "a114", "a115", "a116", "a117", "a118", "a119", "a120", "a121", "a122", "a123", "a124", "a125", "a126", "a127", "a128", "a129",
      "a130", "a131", "a132", "a133", "a134", "a135", "a136", "a137", "a138", "a139", "a140", "a141", "a142", "a143", "a144", "a145", "a146", "a147", "a148", "a149", "a150", "a151", "s8", "s9",
      "s10", "s11", "s12", "s13", "s14", "s15", "s16", "s17", "s18", "s19", "s20", "s21", "s22", "s23", "s24", "s25", "s26", "s27", "s28", "s29", "s30", "s31", "s35", "s36",
      "s37", "s38", "s39", "s40", "s41", "s42", "s43", "s44", "s45", "s46", "s47", "s48", "s49", "s50", "s51", "s52", "s53", "s54", "s55", "s56", "s57", "s58", "s59", "s60",
      "s61", "s62", "s63", "s64", "s65", "s66", "s67", "s68", "s69", "s70", "s71", "s72", "s73", "s74", "s75", "s76", "s77", "s78", "s79", "s80", "s81", "s82", "s83", "s84",
      "s85", "s86", "s87", "s88", "s89", "s90", "s91", "s92", "s93", "s94", "s95", "vcc", "scc", "memory");
  if (__builtin_expect(flag != 0, 0)) {
    asm volatile("s_waitcnt vmcnt(0)" ::: "memory");
    const int lane = threadIdx.x & 63, wave = threadIdx.x >> 6;
    const size_t head = blockIdx.x;
#pragma unroll 1
    for (int qb = 0; qb < 4; ++qb) {
      if ((flag >> qb) & 1) {
        const size_t row0 = head * 256 + wave * 64 + qb * 16;
        redo_rows((const unsigned short*)Q + row0 * 128, (const unsigned short*)K + head * (1024 * 128),
                  (const unsigned short*)V + head * (1024 * 128), (unsigned short*)O + row0 * 128, lane);
      }
    }
  }
}

extern "C" void kernel_launch(void* const* d_in, const int* in_sizes, int n_in, void* d_out, int out_size, void* d_ws, size_t ws_size, hipStream_t stream) {
  attn_fwd_kernel<<<256, 256, 0, stream>>>(d_in[0], d_in[1], d_in[2], d_out);
}
```
